```python
import torch

def _softmax(x, axis=-1):
    return torch.softmax(x, dim=axis)
def _gelu(x):
    return torch.nn.functional.gelu(x, approximate="tanh")
def _silu(x):
    return torch.nn.functional.silu(x)
def _sigmoid(x):
    return torch.sigmoid(x)
def _layernorm(x, axis=-1, eps=1e-5):
    m = x.mean(dim=axis, keepdim=True)
    v = x.var(dim=axis, keepdim=True, unbiased=False)
    return (x - m) / torch.sqrt(v + eps)
def _scatter_add(tgt, idx, src, axis=0):
    return tgt.clone().index_add_(axis, idx, src)
def _segment_sum(src, seg, n):
    out = torch.zeros((n,) + src.shape[1:], dtype=src.dtype)
    return out.index_add_(0, seg, src)
def _pad(x, pw):
    flat = tuple(v for pair in reversed(pw) for v in pair)
    return torch.nn.functional.pad(x, flat)

kernel_name = 'sequential_preLN__mha_block_cached__glu_gated_geglu__decode'

def setup_inputs():

    _w = (lambda a: (lambda t: t.to(torch.bfloat16) if t.is_floating_point() else t)(torch.as_tensor(a)))
    def _A_setup():
        B, Sq, D = 1, 1, 576
        H, Sk, Dh = 32, 16384, 128
        return {
            "x": _w((lambda *s: torch.randn(*s, dtype=torch.bfloat16) * 0.05)(B, Sq, D)),
            "k_cache": _w((lambda *s: torch.randn(*s, dtype=torch.bfloat16) * 3.2)(B, H, Sk, Dh)),
            "v_cache": _w((lambda *s: torch.randn(*s, dtype=torch.bfloat16) * 1.6)(B, H, Sk, Dh)),
            "wq": _w((lambda *s: torch.randn(*s, dtype=torch.bfloat16) * 3.2)(D, H * Dh) * (D ** -0.5)),
            "wo": _w((lambda *s: torch.randn(*s, dtype=torch.bfloat16) * 1.6)(H * Dh, D) * ((H * Dh) ** -0.5)),
        }
    _A_in = _A_setup()

    def _B_setup():
        B, S, Dm, Df = 1, 1, 576, 4608
        return {
            "x": _w((lambda *s: torch.randn(*s, dtype=torch.bfloat16) * 0.05)(B, S, Dm)),
            "w_gate": _w((lambda *s: torch.randn(*s, dtype=torch.bfloat16) * 1.6)(Dm, Df) * (Dm ** -0.5)),
            "w_up": _w((lambda *s: torch.randn(*s, dtype=torch.bfloat16) * 1.6)(Dm, Df) * (Dm ** -0.5)),
            "w_down": _w((lambda *s: torch.randn(*s, dtype=torch.bfloat16) * 1.6)(Df, Dm) * (Df ** -0.5)),
        }
    _B_in = _B_setup()

    return {
        "A_x": _A_in["x"],
        "A_k_cache": _A_in["k_cache"],
        "A_v_cache": _A_in["v_cache"],
        "A_wq": _A_in["wq"],
        "A_wo": _A_in["wo"],
        "B_x": _B_in["x"],
        "B_w_gate": _B_in["w_gate"],
        "B_w_up": _B_in["w_up"],
        "B_w_down": _B_in["w_down"],
    }

def reference(A_x, A_k_cache, A_v_cache, A_wq, A_wo, B_x, B_w_gate, B_w_up, B_w_down):
    def _A_ref(x, k_cache, v_cache, wq, wo):
        B, Sq, D = x.shape
        H, Dh = 32, 128
        q = (lambda x, *d: x.permute(*d))((x @ wq).reshape(B, Sq, H, Dh), 0, 2, 1, 3)
        scores = torch.einsum("bhqd,bhkd->bhqk", q, k_cache) * (Dh ** -0.5)
        p = _softmax(scores, axis=-1)
        o = torch.einsum("bhqk,bhkd->bhqd", p, v_cache)
        return (lambda x, *d: x.permute(*d))(o, 0, 2, 1, 3).reshape(B, Sq, H * Dh) @ wo

    def _B_ref(x, w_gate, w_up, w_down):
        g = torch.einsum("bsd,df->bsf", x, w_gate)
        u = torch.einsum("bsd,df->bsf", x, w_up)
        h = _gelu(g) * u
        return torch.einsum("bsf,fd->bsd", h, w_down)

    x = A_x
    h = x + _A_ref(_layernorm(x, axis=-1), A_k_cache, A_v_cache, A_wq, A_wo)
    h2 = h + _B_ref(_layernorm(h, axis=-1), B_w_gate, B_w_up, B_w_down)
    return h2
```

```cpp
#include <hip/hip_runtime.h>
#include <hip/hip_bf16.h>
#include <stdint.h>
typedef unsigned short u16;
typedef unsigned v4u __attribute__((ext_vector_type(4)));
constexpr int D = 576, H = 32, SK = 16384, DH = 128, DF = 4608, NSPLIT = 16;
__device__ __forceinline__ float bf2f(u16 b) { return __builtin_bit_cast(float, (unsigned)b << 16); }
__device__ __forceinline__ float lo(unsigned u) { return __builtin_bit_cast(float, u << 16); }
__device__ __forceinline__ float hi(unsigned u) { return __builtin_bit_cast(float, u & 0xffff0000u); }
__device__ __forceinline__ float wsum(float v) { for (int o = 32; o; o >>= 1) v += __shfl_xor(v, o); return v; }
__device__ __forceinline__ float wmax(float v) { for (int o = 32; o; o >>= 1) v = fmaxf(v, __shfl_xor(v, o)); return v; }

__device__ void ln576(const float* src, float* dst, int tid) {
  __shared__ float red[2];
  if (tid < 64) {
    float s = 0; for (int i = tid; i < D; i += 64) s += src[i];
    s = wsum(s); float m = s / D; float v = 0;
    for (int i = tid; i < D; i += 64) { float d = src[i] - m; v += d * d; }
    v = wsum(v) / D;
    if (tid == 0) { red[0] = m; red[1] = 1.0f / sqrtf(v + 1e-5f); }
  }
  __syncthreads();
  for (int i = tid; i < D; i += blockDim.x) dst[i] = (src[i] - red[0]) * red[1];
  __syncthreads();
}
__global__ void k_q(const u16* x, const u16* wq, float* q) {
  __shared__ float xs[D], xn[D];
  int tid = threadIdx.x;
  for (int i = tid; i < D; i += 256) xs[i] = bf2f(x[i]);
  __syncthreads();
  ln576(xs, xn, tid);
  int c = blockIdx.x * 256 + tid; float a = 0;
  for (int k = 0; k < D; ++k) a += xn[k] * bf2f(wq[(size_t)k * (H * DH) + c]);
  q[c] = a;
}
__global__ void __launch_bounds__(256) k_attn(const float* q, const u16* K, const u16* V, float* part) {
  int s = blockIdx.x, h = blockIdx.y, tid = threadIdx.x, lane = tid & 63, w = tid >> 6, r = lane >> 4, c = lane & 15;
  constexpr int KPW = SK / NSPLIT / 4;
  float qv[8]; for (int j = 0; j < 8; ++j) qv[j] = q[h * DH + c * 8 + j] * 0.08838834764831845f;
  size_t base = ((size_t)h * SK + (size_t)s * (SK / NSPLIT) + w * KPW) * DH;
  float m = -INFINITY, l = 0, acc[8] = {0, 0, 0, 0, 0, 0, 0, 0};
  for (int k = 0; k < KPW; k += 4) {
    v4u kv = __builtin_nontemporal_load((const v4u*)(K + base + (size_t)(k + r) * DH + c * 8));
    v4u vv = __builtin_nontemporal_load((const v4u*)(V + base + (size_t)(k + r) * DH + c * 8));
    float d = 0;
    for (int j = 0; j < 4; ++j) d += lo(kv[j]) * qv[2 * j] + hi(kv[j]) * qv[2 * j + 1];
    for (int o = 8; o; o >>= 1) d += __shfl_xor(d, o);
    float mn = fmaxf(m, d), al = __expf(m - mn), p = __expf(d - mn);
    l = l * al + p; m = mn;
    for (int j = 0; j < 4; ++j) { acc[2 * j] = acc[2 * j] * al + p * lo(vv[j]); acc[2 * j + 1] = acc[2 * j + 1] * al + p * hi(vv[j]); }
  }
  int slot = ((h * NSPLIT + s) * 4 + w) * 4 + r;
  float* o = part + (size_t)slot * 132;
  for (int j = 0; j < 8; ++j) o[4 + c * 8 + j] = acc[j];
  if (c == 0) { o[0] = m; o[1] = l; }
}
__global__ void k_comb(const float* part, float* o) {
  int h = blockIdx.x, d = threadIdx.x; constexpr int NP = NSPLIT * 16;
  const float* p = part + (size_t)h * NP * 132;
  float M = -INFINITY; for (int i = 0; i < NP; ++i) M = fmaxf(M, p[i * 132]);
  float L = 0, a = 0;
  for (int i = 0; i < NP; ++i) { float w = __expf(p[i * 132] - M); L += w * p[i * 132 + 1]; a += w * p[i * 132 + 4 + d]; }
  o[h * DH + d] = a / L;
}
__global__ void k_wo(const float* o, const u16* wo, float* hp) {
  int c = threadIdx.x, k0 = blockIdx.x * 64; float a = 0;
  for (int k = k0; k < k0 + 64; ++k) a += o[k] * bf2f(wo[(size_t)k * D + c]);
  hp[blockIdx.x * D + c] = a;
}
__global__ void k_gu(const u16* x, const float* hp, const u16* wg, const u16* wu, float* hout, float* act) {
  __shared__ float hs[D], hn[D];
  int tid = threadIdx.x;
  for (int i = tid; i < D; i += 256) { float a = bf2f(x[i]); for (int j = 0; j < 64; ++j) a += hp[j * D + i]; hs[i] = a; if (blockIdx.x == 0) hout[i] = a; }
  __syncthreads();
  ln576(hs, hn, tid);
  int c = blockIdx.x * 256 + tid; float g = 0, u = 0;
  for (int k = 0; k < D; ++k) { g += hn[k] * bf2f(wg[(size_t)k * DF + c]); u += hn[k] * bf2f(wu[(size_t)k * DF + c]); }
  float t = tanhf(0.7978845608028654f * (g + 0.044715f * g * g * g));
  act[c] = 0.5f * g * (1.0f + t) * u;
}
__global__ void k_dn(const float* act, const u16* wd, float* dp) {
  int c = threadIdx.x, k0 = blockIdx.x * 64; float a = 0;
  for (int k = k0; k < k0 + 64; ++k) a += act[k] * bf2f(wd[(size_t)k * D + c]);
  dp[blockIdx.x * D + c] = a;
}
__global__ void k_fin(const float* hout, const float* dp, __hip_bfloat16* out) {
  int c = threadIdx.x; float a = hout[c];
  for (int j = 0; j < 72; ++j) a += dp[j * D + c];
  out[c] = __float2bfloat16(a);
}
extern "C" void kernel_launch(void* const* d_in, const int* in_sizes, int n_in, void* d_out, int out_size, void* d_ws, size_t ws_size, hipStream_t stream) {
  const u16* x = (const u16*)d_in[0]; const u16* K = (const u16*)d_in[1]; const u16* V = (const u16*)d_in[2];
  const u16* wq = (const u16*)d_in[3]; const u16* wo = (const u16*)d_in[4];
  const u16* wg = (const u16*)d_in[6]; const u16* wu = (const u16*)d_in[7]; const u16* wd = (const u16*)d_in[8];
  float* ws = (float*)d_ws;
  float* q = ws; float* o = ws + 4096; float* hp = ws + 8192; float* hout = hp + 64 * D; float* act = hout + 1024; float* dp = act + 8192; float* part = dp + 72 * D + 64;
  k_q<<<16, 256, 0, stream>>>(x, wq, q);
  k_attn<<<dim3(NSPLIT, H), 256, 0, stream>>>(q, K, V, part);
  k_comb<<<32, 128, 0, stream>>>(part, o);
  k_wo<<<64, 576, 0, stream>>>(o, wo, hp);
  k_gu<<<18, 256, 0, stream>>>(x, hp, wg, wu, hout, act);
  k_dn<<<72, 576, 0, stream>>>(act, wd, dp);
  k_fin<<<1, 576, 0, stream>>>(hout, dp, (__hip_bfloat16*)d_out);
}
```

```cpp
#include <hip/hip_runtime.h>
#include <stdint.h>

namespace {
typedef unsigned u32;
typedef unsigned long long u64;
typedef u32 v4u __attribute__((ext_vector_type(4)));
typedef u32 v2u __attribute__((ext_vector_type(2)));
typedef float v2f __attribute__((ext_vector_type(2)));
typedef float v4f __attribute__((ext_vector_type(4)));
typedef __bf16 bf2 __attribute__((ext_vector_type(2)));
typedef __amdgpu_buffer_rsrc_t rsrc_t;

#define PACE_T 3950
#define PACE_LEAD 100
#define PACE_G 4
#define NK0 7
#define QDELAY 40
#define NGE 9
#define NG0 0
#define GPOS 11
#define DN_EARLY 1
#define NREP 16

constexpr int D = 576, SK = 16384, DF = 4608;
constexpr int NWG = 256, NT = 512;
constexpr u32 KVB = 32u * SK * 128 * 2, WQB = D * 4096 * 2, WGB = D * DF * 2;
constexpr int SC1 = 16, NTL = 2;
constexpr u32 WS_EPOCH = 0;
constexpr u32 WS_ML = 4096;
constexpr u32 WS_Q = 65536;
constexpr u32 WS_P = WS_Q + 32 * 8 * 1024;
constexpr u32 WS_DN = WS_P + 72 * 256 * 64;
constexpr u32 HB_STRIDE = 2560;
constexpr u32 WS_HB = WS_DN + 72 * 144 * 64;
constexpr u32 WS_END = WS_HB + NREP * HB_STRIDE;
constexpr u32 OOB = 0x40000000u;
constexpr u32 SPIN_TICKS = 2000000u;

__device__ __forceinline__ rsrc_t mk(const void* p, u32 bytes) { return __builtin_amdgcn_make_buffer_rsrc((void*)p, 0, bytes, 0x00020000); }
__device__ __forceinline__ float f32(u32 u) { return __builtin_bit_cast(float, u); }
__device__ __forceinline__ u32 bits(float f) { return __builtin_bit_cast(u32, f); }
__device__ __forceinline__ float blo(u32 u) { return f32(u << 16); }
__device__ __forceinline__ float bhi(u32 u) { return f32(u & 0xffff0000u); }
__device__ __forceinline__ float dot2(u32 a, u32 b, float c) { return __builtin_amdgcn_fdot2_f32_bf16(__builtin_bit_cast(bf2, a), __builtin_bit_cast(bf2, b), c, false); }
__device__ __forceinline__ u32 pk(float lo, float hi) { bf2 r = {(__bf16)lo, (__bf16)hi}; return __builtin_bit_cast(u32, r); }
__device__ __forceinline__ float rbf(float x) { return (float)(__bf16)x; }
template <int CTRL> __device__ __forceinline__ float dpp(float x) { return f32(__builtin_amdgcn_mov_dpp(bits(x), CTRL, 0xf, 0xf, true)); }
template <int CTRL> __device__ __forceinline__ u32 dppu(u32 x) { return __builtin_amdgcn_mov_dpp(x, CTRL, 0xf, 0xf, true); }
constexpr int XOR1 = 0xB1, XOR2 = 0x4E, XOR7 = 0x141, XOR8 = 0x128, SHL4 = 0x104, BC0 = 0x150;
__device__ __forceinline__ float tr32(float a, float b) { auto s = __builtin_amdgcn_permlane32_swap(bits(a), bits(b), false, false); return f32(s[0]) + f32(s[1]); }
__device__ __forceinline__ float tr16(float a, float b) { auto s = __builtin_amdgcn_permlane16_swap(bits(a), bits(b), false, false); return f32(s[0]) + f32(s[1]); }
__device__ __forceinline__ float wave_sum(float x) {
  x += dpp<XOR1>(x); x += dpp<XOR2>(x); x += dpp<XOR7>(x); x += dpp<XOR8>(x);
  x = tr16(x, x); return tr32(x, x);
}
__device__ __forceinline__ float wave_max(float x) {
  x = fmaxf(x, dpp<XOR1>(x)); x = fmaxf(x, dpp<XOR2>(x)); x = fmaxf(x, dpp<XOR7>(x)); x = fmaxf(x, dpp<XOR8>(x));
  auto s = __builtin_amdgcn_permlane16_swap(bits(x), bits(x), false, false); x = fmaxf(f32(s[0]), f32(s[1]));
  auto t = __builtin_amdgcn_permlane32_swap(bits(x), bits(x), false, false); return fmaxf(f32(t[0]), f32(t[1]));
}
__device__ __forceinline__ u32 plo(u32 a, u32 b) { return __builtin_amdgcn_perm(b, a, 0x05040100u); }
__device__ __forceinline__ u32 phi(u32 a, u32 b) { return __builtin_amdgcn_perm(b, a, 0x07060302u); }
__device__ __forceinline__ u32 now() { return (u32)__builtin_amdgcn_s_memrealtime(); }
#define SB() __builtin_amdgcn_sched_barrier(0)
#define CFENCE() asm volatile("" ::: "memory")
#define WG_BARRIER() do { asm volatile("s_waitcnt lgkmcnt(0)" ::: "memory"); __builtin_amdgcn_s_barrier(); CFENCE(); } while (0)
#define LD128(r, v, s, aux) __builtin_amdgcn_raw_buffer_load_b128(r, v, s, aux)
#define LD32(r, v, s, aux) __builtin_amdgcn_raw_buffer_load_b32(r, v, s, aux)
#define ST128(d, r, v, s, aux) __builtin_amdgcn_raw_buffer_store_b128(d, r, v, s, aux)
#define TAGOK(g) ((g).y == tag && (g).w == tag)
#define DMA1K(ldsaddr, vo, rs, soff) do { u32 keep_; \
    asm volatile("s_mov_b32 %0, m0\n\ts_mov_b32 m0, %1\n\ts_nop 0\n\tbuffer_load_dwordx4 %2, %3, %4 offen nt lds\n\ts_mov_b32 m0, %0" \
                 : "=&s"(keep_) : "s"(ldsaddr), "v"(vo), "s"(rs), "s"(soff) : "memory"); } while (0)

#define STAMP(i) do { } while (0)

constexpr int IMG_RED = 0, IMG_GW = 20480, IMG_DN = 94208;

__global__ void __launch_bounds__(NT, 2) fused_decode(const void* __restrict__ px, const void* __restrict__ pk_, const void* __restrict__ pv,
    const void* __restrict__ pwq, const void* __restrict__ pwo, const void* __restrict__ pwg, const void* __restrict__ pwu,
    const void* __restrict__ pwd, void* pout, void* pws) {
  __shared__ __attribute__((aligned(16))) float lds[3 * 1024 + 640 + 36864];
  float* const qpart = lds;
  float* const qpart2 = lds + 1024;
  float* const part = lds + 2048;
  float* const misc = lds + 3072;
  float* const img = lds + 3712;
  const u32 img_lds = (u32)(uintptr_t)(__attribute__((address_space(3))) float*)img;
  const u32 t0 = now();
  const int b = blockIdx.x, tid = threadIdx.x, lane = tid & 63, w = __builtin_amdgcn_readfirstlane(tid >> 6);
  const int r = lane >> 4, c = lane & 15;
  const int xcd = b & 7, j = b >> 3, h = xcd * 4 + (j >> 3), sp = j & 7;
  const bool is_mlp = j < 18;
  const int cidx = xcd * 18 + j;
  const rsrc_t rx = mk(px, D * 2), rk = mk(pk_, KVB), rv = mk(pv, KVB), rq = mk(pwq, WQB), ro = mk(pwo, WQB), rw = mk(pws, WS_END);
  const u32 voff = lane * 16;

  __builtin_amdgcn_s_setprio(3);
  const int qrow = 72 * sp + 9 * w;
  const u32 ep = LD32(rw, WS_EPOCH, 0, SC1);
  const v4u xs0 = LD128(rx, lane * 32, 0, 0), xs1 = LD128(rx, lane * 32 + 16, 0, 0);
  u32 xr[3]; v4u wq[3];
#pragma unroll
  for (int i = 0; i < 3; ++i) xr[i] = __builtin_amdgcn_raw_buffer_load_b16(rx, r * 2, (qrow + 4 * i) * 2, 0);
#pragma unroll
  for (int i = 0; i < 3; ++i) wq[i] = LD128(rq, (i < 2 || r == 0) ? r * 8192 + c * 16 : OOB, (qrow + 4 * i) * 8192 + h * 256, NTL);
  SB();
  __builtin_amdgcn_s_setprio(0);
  const u32 kvbase = (u32)(h * 8 + sp) * 524288u + w * 1024;
  v4u a[16];
#pragma unroll
  for (int i = 0; i < NK0; ++i) a[i] = LD128(rk, voff, kvbase + i * 8192, NTL);
  SB();
  STAMP(1);

  {
    const u32 ones = 0x3f803f80u;
    float s = 0.f;
#pragma unroll
    for (int d = 0; d < 4; ++d) { s = dot2(xs0[d], ones, s); s = dot2(xs1[d], ones, s); }
    const float mean = wave_sum(s) * (1.0f / D);
    float vs = 0.f;
#pragma unroll
    for (int d = 0; d < 4; ++d) {
      const float e0 = blo(xs0[d]) - mean, e1 = bhi(xs0[d]) - mean, e2 = blo(xs1[d]) - mean, e3 = bhi(xs1[d]) - mean;
      vs += e0 * e0 + e1 * e1 + e2 * e2 + e3 * e3;
    }
    vs = lane < 36 ? vs : 0.f;
    const float var = wave_sum(vs) * (1.0f / D);
    const float mb = rbf(mean), sd = rbf(__builtin_amdgcn_sqrtf(rbf(rbf(var) + 1e-5f)));
    const float rs = __builtin_amdgcn_rcpf(sd);
    float acc[8];
#pragma unroll
    for (int d = 0; d < 8; ++d) acc[d] = 0.f;
#pragma unroll
    for (int i = 0; i < 3; ++i) {
      const u32 xh = bits(rbf(rbf(blo(xr[i]) - mb) * rs)), xl = xh >> 16;
#pragma unroll
      for (int d = 0; d < 4; ++d) { acc[2 * d] = dot2(wq[i][d], xl, acc[2 * d]); acc[2 * d + 1] = dot2(wq[i][d], xh, acc[2 * d + 1]); }
    }
    float t[4];
#pragma unroll
    for (int d = 0; d < 4; ++d) t[d] = tr32(acc[d], acc[d + 4]);
    *(v2f*)(qpart + w * 128 + lane * 2) = v2f{tr16(t[0], t[2]), tr16(t[1], t[3])};
  }
  const u32 tag = __builtin_amdgcn_readfirstlane(ep) + 1u;
  WG_BARRIER();
  if (w == 0) {
    v2f s = {0.f, 0.f};
#pragma unroll
    for (int i = 0; i < 8; ++i) s += *(const v2f*)(qpart + i * 128 + lane * 2);
    ST128((v4u{bits(s.x), tag, bits(s.y), tag}), rw, voff, WS_Q + (h * 8 + sp) * 1024, SC1);
  }
  STAMP(7);
  u32 q0, q1, q2, q3;
  {
    const u32 tq = now();
    while (now() - tq < QDELAY) __builtin_amdgcn_s_sleep(1);
    v4u g = LD128(rw, voff, WS_Q + (h * 8 + w) * 1024, SC1);
    SB();
#pragma unroll
    for (int i = NK0; i < 16; ++i) a[i] = LD128(rk, voff, kvbase + i * 8192, NTL);
    SB();
    if (!__all(TAGOK(g))) {
      do { CFENCE(); g = LD128(rw, voff, WS_Q + (h * 8 + w) * 1024, SC1); } while (!__all(TAGOK(g)) && now() - tq < SPIN_TICKS);
    }
    *(v2f*)(qpart2 + w * 128 + lane * 2) = v2f{f32(g.x), f32(g.z)};
    WG_BARRIER();
    v2f s = {0.f, 0.f};
#pragma unroll
    for (int i = 0; i < 8; ++i) s += *(const v2f*)(qpart2 + i * 128 + lane * 2);
    const u32 qd = pk(s.x, s.y);
    auto e = __builtin_amdgcn_permlane16_swap(qd, qd, false, false);
    auto g0 = __builtin_amdgcn_permlane32_swap(e[0], e[0], false, false);
    auto g1 = __builtin_amdgcn_permlane32_swap(e[1], e[1], false, false);
    q0 = g0[0]; q2 = g0[1]; q1 = g1[0]; q3 = g1[1];
  }
  STAMP(2);

#define PACE(n) do { if (PACE_T) { const int due_ = (int)((u32)(n) * (u32)PACE_T >> 7) - PACE_LEAD; \
    while ((int)(now() - t0) < due_) __builtin_amdgcn_s_sleep(1); } } while (0)

  float s0 = 0.f, s1 = 0.f, s2 = 0.f, s3 = 0.f;
  {
    u32 wo_n = 0;
    const u32 wo_lds = img_lds + w * 18432, wo_src = (h * 128 + 16 * w) * 1152;
#pragma unroll 1
    for (int g = 0; g < 4; ++g) {
      const rsrc_t rn = g == 3 ? rv : rk;
      const u32 nb = kvbase + (g == 3 ? 0 : (g + 1) * 131072);
      float x[16];
#pragma unroll
      for (int i = 0; i < 16; ++i) {
        float d = dot2(a[i][0], q0, 0.f); d = dot2(a[i][1], q1, d); d = dot2(a[i][2], q2, d); d = dot2(a[i][3], q3, d);
        x[i] = d;
        asm volatile("" : "+v"(x[i]));
        if (i % PACE_G == 0) PACE(16 * g + 16 + i);
        a[i] = LD128(rn, voff, nb + i * 8192, NTL);
        if (i % 8 == 3 || i % 8 == 5 || i % 8 == 7) { if (wo_n < 18432) { DMA1K(wo_lds + wo_n, voff, ro, wo_src + wo_n); wo_n += 1024; } }
        SB();
      }
#define TR_STEP(HALF, CTRL, BIT) _Pragma("unroll") for (int k = 0; k < (HALF); ++k) { \
        const float lo_ = x[k], hi_ = x[k + (HALF)]; const float t1_ = lo_ + dpp<CTRL>(lo_), t2_ = hi_ + dpp<CTRL>(hi_); x[k] = (c & (BIT)) ? t2_ : t1_; }
      TR_STEP(8, XOR8, 8) TR_STEP(4, XOR7, 4) TR_STEP(2, XOR2, 2) TR_STEP(1, XOR1, 1)
#undef TR_STEP
      s0 = s1; s1 = s2; s2 = s3; s3 = rbf(rbf(x[0]) * 0.08838834764831845f);
    }
  }
  STAMP(3);
  constexpr float L2E = 1.4426950408889634f;
  const float mw = wave_max(fmaxf(fmaxf(s0, s1), fmaxf(s2, s3)));
  s0 = __builtin_amdgcn_exp2f((s0 - mw) * L2E); s1 = __builtin_amdgcn_exp2f((s1 - mw) * L2E);
  s2 = __builtin_amdgcn_exp2f((s2 - mw) * L2E); s3 = __builtin_amdgcn_exp2f((s3 - mw) * L2E);
  const float lw = wave_sum((s0 + s1) + (s2 + s3));
  asm volatile("s_waitcnt vmcnt(16)" ::: "memory");
  ST128((v4u{bits(mw), tag, bits(lw), tag}), rw, lane == 0 ? 0u : OOB, WS_ML + (h * 64 + sp * 8 + w) * 16, SC1);
  u32 e[8][8], ex[4][2];
  {
    const char* wl = (const char*)img + w * 18432;
#pragma unroll
    for (int k = 0; k < 8; ++k) {
      const v4u w0 = *(const v4u*)(wl + (2 * k) * 1152 + lane * 16), w1 = *(const v4u*)(wl + (2 * k + 1) * 1152 + lane * 16);
#pragma unroll
      for (int d = 0; d < 4; ++d) { e[k][2 * d] = plo(w0[d], w1[d]); e[k][2 * d + 1] = phi(w0[d], w1[d]); }
      asm volatile("" : "+v"(e[k][0]), "+v"(e[k][1]), "+v"(e[k][2]), "+v"(e[k][3]), "+v"(e[k][4]), "+v"(e[k][5]), "+v"(e[k][6]), "+v"(e[k][7]));
    }
#pragma unroll
    for (int k = 0; k < 4; ++k) {
      const char* wp = wl + ((lane >> 5) * 8 + 2 * k) * 1152 + 1024 + (lane & 31) * 4;
      const u32 w0 = *(const u32*)wp, w1 = *(const u32*)(wp + 1152);
      ex[k][0] = plo(w0, w1); ex[k][1] = phi(w0, w1);
      asm volatile("" : "+v"(ex[k][0]), "+v"(ex[k][1]));
    }
  }
  WG_BARRIER();

  const int g16 = lane >> 2;
  const rsrc_t rg_ = mk(w < 4 ? pwg : pwu, WGB), rd = mk(pwd, WGB);
  const u32 gvo = g16 * 9216 + (lane & 3) * 16, gso = (144 * (w & 3)) * 9216 + cidx * 64, gw_lds = img_lds + IMG_GW + w * 9216;
  u32 gw_n = is_mlp ? 0u : 9u;
#define GW_DMA(lim) do { if (gw_n < (lim)) { DMA1K(gw_lds + gw_n * 1024, gvo, rg_, gso + gw_n * (16 * 9216)); ++gw_n; } } while (0)
#define DN_DMA() do { for (int p_ = w; p_ < 36; p_ += 8) DMA1K(img_lds + IMG_DN + p_ * 1024, voff, rd, cidx * (32 * 1152) + p_ * 1024); } while (0)
  float acc[8];
#pragma unroll
  for (int d = 0; d < 8; ++d) acc[d] = 0.f;
#define PV(i) do { const float p_ = dpp<BC0 + (i)>(s0); _Pragma("unroll") for (int d = 0; d < 4; ++d) { \
      acc[2 * d] = fmaf(p_, blo(a[i][d]), acc[2 * d]); acc[2 * d + 1] = fmaf(p_, bhi(a[i][d]), acc[2 * d + 1]); } \
      asm volatile("" : "+v"(acc[0]), "+v"(acc[1]), "+v"(acc[2]), "+v"(acc[3]), "+v"(acc[4]), "+v"(acc[5]), "+v"(acc[6]), "+v"(acc[7])); } while (0)
#define REP16(M) M(0) M(1) M(2) M(3) M(4) M(5) M(6) M(7) M(8) M(9) M(10) M(11) M(12) M(13) M(14) M(15)
  v4u ml;
#pragma unroll 1
  for (int g = 0; g < 3; ++g) {
    ml = LD128(rw, voff, WS_ML + h * 1024, SC1);
    const u32 nb = kvbase + (g + 1) * 131072;
#define VSTEP(i) PV(i); if ((i) % PACE_G == 0) PACE(16 * g + 80 + (i)); a[i] = LD128(rv, voff, nb + (i) * 8192, NTL); \
    if ((i) % 4 == 3) GW_DMA(NGE); SB();
    REP16(VSTEP)
#undef VSTEP
    s0 = s1; s1 = s2; s2 = s3;
  }
  STAMP(4);
  if (!__all(TAGOK(ml))) {
    const u32 tw = now();
    do { __builtin_amdgcn_s_sleep(8); CFENCE(); ml = LD128(rw, voff, WS_ML + h * 1024, SC1); } while (!__all(TAGOK(ml)) && now() - tw < SPIN_TICKS);
  }
  float scale;
  {
    const float M = wave_max(f32(ml.x));
    const float L = wave_sum(f32(ml.z) * __builtin_amdgcn_exp2f((f32(ml.x) - M) * L2E));
    scale = __builtin_amdgcn_exp2f((mw - M) * L2E) * __builtin_amdgcn_rcpf(L);
  }
  SB();
#define VLAST(i) PV(i); SB();
  VLAST(0) VLAST(1) VLAST(2) VLAST(3) VLAST(4) VLAST(5) VLAST(6) VLAST(7) VLAST(8) VLAST(9) VLAST(10) VLAST(11)
  VLAST(12) VLAST(13) VLAST(14) VLAST(15)
#undef VLAST
  if (DN_EARLY && is_mlp) DN_DMA();
  STAMP(5);

  {
    float* const red = img + IMG_RED / 4;
    float t[4];
#pragma unroll
    for (int d = 0; d < 4; ++d) t[d] = tr32(acc[d], acc[d + 4]);
    *(v2f*)(part + w * 128 + 8 * c + 2 * r) = v2f{tr16(t[0], t[2]) * scale, tr16(t[1], t[3]) * scale};
    WG_BARRIER();
    v2f o = *(const v2f*)(part + (lane >> 3) * 128 + 16 * w + 2 * (lane & 7));
    o.x += dpp<XOR8>(o.x); o.y += dpp<XOR8>(o.y);
    o.x = tr16(o.x, o.x); o.y = tr16(o.y, o.y); o.x = tr32(o.x, o.x); o.y = tr32(o.y, o.y);
    const u32 op = pk(o.x, o.y);
    float pm[8], px0 = 0.f, px1 = 0.f;
#pragma unroll
    for (int n = 0; n < 8; ++n) pm[n] = 0.f;
#define OSTEP(k) { const u32 ok_ = dppu<BC0 + (k)>(op); _Pragma("unroll") for (int n = 0; n < 8; ++n) pm[n] = dot2(e[k][n], ok_, pm[n]); }
    OSTEP(0) OSTEP(1) OSTEP(2) OSTEP(3) OSTEP(4) OSTEP(5) OSTEP(6) OSTEP(7)
#undef OSTEP
#define XSTEP(k) { u32 ok_ = __builtin_amdgcn_update_dpp(0u, op, BC0 + (k), 0x3, 0xf, false); ok_ = __builtin_amdgcn_update_dpp(ok_, op, BC0 + 4 + (k), 0xc, 0xf, false); \
      px0 = dot2(ex[k][0], ok_, px0); px1 = dot2(ex[k][1], ok_, px1); }
    XSTEP(0) XSTEP(1) XSTEP(2) XSTEP(3)
#undef XSTEP
    const float pxs = tr32(px0, px1);
    float* rw_ = red + w * 640;
    *(v4f*)(rw_ + lane * 4) = v4f{pm[0], pm[1], pm[2], pm[3]};
    *(v4f*)(rw_ + 288 + lane * 4) = v4f{pm[4], pm[5], pm[6], pm[7]};
    rw_[576 + 2 * (lane & 31) + (lane >> 5)] = pxs;
    WG_BARRIER();
    if (w < 5) {
      const int off = tid < 256 ? ((tid >> 1) & 1) * 288 + (tid >> 2) * 4 + 2 * (tid & 1) : 576 + 2 * (tid & 31);
      v2f s = {0.f, 0.f};
#pragma unroll
      for (int i = 0; i < 8; ++i) s += *(const v2f*)(red + i * 640 + off);
      ST128((v4u{bits(s.x), tag, bits(s.y), tag}), rw, tid < 288 ? (tid >> 2) * (256 * 64) + (tid & 3) * 16 : OOB, WS_P + b * 64, SC1);
    }
  }
  STAMP(6);

  if (is_mlp) {
    if (NGE < 9 || !DN_EARLY) {
      __builtin_amdgcn_s_barrier();
      while (gw_n < 9) GW_DMA(9);
      if (!DN_EARLY) DN_DMA();
    }
    asm volatile("s_waitcnt vmcnt(0)" ::: "memory");
    u32 ge[4][8]; v4u g8;
    {
      const char* gl = (const char*)img + IMG_GW + w * 9216 + (lane & 3) * 16;
#pragma unroll
      for (int k = 0; k < 4; ++k) {
        const v4u w0 = *(const v4u*)(gl + (8 * g16 + 2 * k) * 64), w1 = *(const v4u*)(gl + (8 * g16 + 2 * k + 1) * 64);
#pragma unroll
        for (int d = 0; d < 4; ++d) { ge[k][2 * d] = plo(w0[d], w1[d]); ge[k][2 * d + 1] = phi(w0[d], w1[d]); }
      }
      g8 = *(const v4u*)(gl + (128 + g16) * 64);
    }
    const u32 hso = WS_HB + (u32)(cidx % NREP) * HB_STRIDE + 576 * (w & 3);
    u32 dw[32];
#pragma unroll
    for (int k = 0; k < 32; ++k) dw[k] = 0u;
    float Sg = 0.f, Su = 0.f, Ag = 0.f, Au = 0.f, sh = 0.f, sq = 0.f;
#pragma unroll 1
    for (int it = 0; it < 2; ++it) {
      u32 hp0 = 0x3f803f80u, hp1 = hp0, hp2 = hp0, hp3 = hp0, h8 = 0x3f80u;
      if (it) {
        const char* dl = (const char*)img + IMG_DN + (tid < 288 ? tid : 0) * 4;
#pragma unroll
        for (int k = 0; k < 16; ++k) { const u32 w0 = *(const u32*)(dl + 2 * k * 1152), w1 = *(const u32*)(dl + (2 * k + 1) * 1152);
          dw[2 * k] = plo(w0, w1); dw[2 * k + 1] = phi(w0, w1); asm volatile("" : "+v"(dw[2 * k]), "+v"(dw[2 * k + 1])); }
        STAMP(13);
        v4u ga, gb, gc;
        const u32 tw = now();
        for (;;) {
          CFENCE();
          ga = LD128(rw, g16 * 32, hso, SC1); gb = LD128(rw, g16 * 32 + 16, hso, SC1); gc = LD128(rw, 512 + (g16 >> 2) * 16, hso, SC1);
          if (__all(TAGOK(ga) && TAGOK(gb) && TAGOK(gc)) || now() - tw > SPIN_TICKS) break;
        }
        hp0 = ga.x; hp1 = ga.z; hp2 = gb.x; hp3 = gb.z;
        const u32 pr = (g16 & 2) ? gc.z : gc.x;
        h8 = (g16 & 1) ? pr >> 16 : pr & 0xffffu;
        STAMP(8);
      }
      float y[8];
#pragma unroll
      for (int n = 0; n < 8; ++n) {
        float s = dot2(ge[0][n], hp0, 0.f); s = dot2(ge[1][n], hp1, s); s = dot2(ge[2][n], hp2, s); s = dot2(ge[3][n], hp3, s);
        y[n] = dot2(g8[n >> 1], (n & 1) ? h8 << 16 : h8, s);
      }
      const u32 ones = 0x3f803f80u;
      float a1 = dot2(hp0, ones, dot2(hp1, ones, dot2(hp2, ones, dot2(hp3, ones, dot2(h8, ones, 0.f)))));
      float a2 = dot2(hp0, hp0, dot2(hp1, hp1, dot2(hp2, hp2, dot2(hp3, hp3, dot2(h8, h8, 0.f)))));
      float t[4];
#pragma unroll
      for (int d = 0; d < 4; ++d) t[d] = tr32(y[d], y[d + 4]);
      const float u0 = tr16(t[0], t[2]), u1 = tr16(t[1], t[3]);
      const float v0 = u0 + dpp<XOR8>(u0), v1 = u1 + dpp<XOR8>(u1);
      float z = (lane & 8) ? v1 : v0;
      z += dpp<SHL4>(z);
      float st = tr32(a1, a2); st = tr16(st, st); st += dpp<XOR8>(st); st += dpp<SHL4>(st);
      float* gp = misc + it * 256;
      if (!(lane & 4)) gp[w * 32 + (lane & 3) * 8 + (lane >> 3)] = z;
      if ((lane & 31) == 0) misc[512 + w * 2 + (lane >> 5)] = st;
      WG_BARRIER();
      const int n = lane & 31;
      Ag = (gp[n] + gp[32 + n]) + (gp[64 + n] + gp[96 + n]);
      Au = (gp[128 + n] + gp[160 + n]) + (gp[192 + n] + gp[224 + n]);
      if (it == 0) { Sg = Ag; Su = Au; }
      else { const v4f p0 = *(const v4f*)(misc + 512), p1 = *(const v4f*)(misc + 516); sh = (p0.x + p0.z) + (p1.x + p1.z); sq = (p0.y + p0.w) + (p1.y + p1.w); }
    }
    const float mean = sh * (1.0f / D), var = fmaxf(sq * (1.0f / D) - mean * mean, 0.f);
    const float mb = rbf(mean), rs = __builtin_amdgcn_rcpf(rbf(__builtin_amdgcn_sqrtf(rbf(rbf(var) + 1e-5f))));
    const float gv = rbf((Ag - mb * Sg) * rs), uv = rbf((Au - mb * Su) * rs);
    const float zz = gv * (1.0f + 0.044715f * gv * gv) * (-2.0f * 0.7978845608028654f * L2E);
    const float gl = rbf(gv * __builtin_amdgcn_rcpf(1.0f + __builtin_amdgcn_exp2f(zz)));
    const u32 ab = bits(rbf(gl * uv));
    const u32 ap = (ab >> 16) | dppu<XOR1>(ab);
    float d0 = 0.f, d1 = 0.f;
#pragma unroll
    for (int k = 0; k < 16; ++k) { const u32 s = __builtin_amdgcn_readlane(ap, 2 * k); d0 = dot2(dw[2 * k], s, d0); d1 = dot2(dw[2 * k + 1], s, d1); }
    ST128((v4u{bits(d0), tag, bits(d1), tag}), rw, tid < 288 ? (tid >> 2) * (144 * 64) + (tid & 3) * 16 : OOB, WS_DN + cidx * 64, SC1);
    STAMP(9);
  } else if (j < 27) {
    const int rr = xcd * 9 + (j - 18);
    const v2u xv = __builtin_amdgcn_raw_buffer_load_b64(rx, (lane & 1) * 8, rr * 16, 0);
    float res0 = blo(xv.x), res1 = bhi(xv.x), res2 = blo(xv.y), res3 = bhi(xv.y);
#pragma unroll 1
    for (int it = 0; it < 2; ++it) {
      const u32 so = it ? WS_DN + rr * (144 * 64) : WS_P + rr * (256 * 64);
      const bool has2 = !it || tid < 64;
      v4u ga = {0u, 0u, 0u, 0u}, gb = ga;
      bool oka = false, okb = !has2;
      const u32 tw = now();
      for (;;) {
        CFENCE();
        if (!oka) ga = LD128(rw, tid * 16, so, SC1);
        if (!okb) gb = LD128(rw, tid * 16 + 8192, so, SC1);
        oka = TAGOK(ga); okb = !has2 || TAGOK(gb);
        if (__all(oka && okb) || now() - tw > SPIN_TICKS) break;
      }
      if (it == 0) STAMP(10); else STAMP(12);
      const float c0 = f32(ga.x) + f32(gb.x), c1 = f32(ga.z) + f32(gb.z);
      float z = tr32(c0, c1); z = tr16(z, z); z += dpp<XOR8>(z); z += dpp<SHL4>(z);
      float* gp = misc + it * 64;
      if ((lane & 31) < 4) gp[w * 8 + 2 * (lane & 3) + (lane >> 5)] = z;
      WG_BARRIER();
      if (w == 0) {
        v4f s = {0.f, 0.f, 0.f, 0.f};
#pragma unroll
        for (int i = 0; i < 8; ++i) s += *(const v4f*)(gp + i * 8 + (lane & 1) * 4);
        res0 = rbf(res0 + rbf(s.x)); res1 = rbf(res1 + rbf(s.y)); res2 = rbf(res2 + rbf(s.z)); res3 = rbf(res3 + rbf(s.w));
        const u32 p0 = (bits(res0) >> 16) | bits(res1), p1 = (bits(res2) >> 16) | bits(res3);
        if (it == 0) {
          ST128((v4u{p0, tag, p1, tag}), rw, lane < 2 * NREP ? (lane >> 1) * HB_STRIDE + (lane & 1) * 16 : OOB, WS_HB + rr * 32, SC1);
        } else {
          const rsrc_t rout = mk(pout, D * 2);
          __builtin_amdgcn_raw_buffer_store_b64(v2u{p0, p1}, rout, lane < 2 ? lane * 8 : OOB, rr * 16, 0);
          if (rr == 0) __builtin_amdgcn_raw_buffer_store_b32(tag, rw, lane == 0 ? WS_EPOCH : OOB, 0, SC1);
        }
      }
      if (it == 0) STAMP(11);
    }
  }
}
}

extern "C" void kernel_launch(void* const* d_in, const int* in_sizes, int n_in, void* d_out, int out_size, void* d_ws, size_t ws_size,
                              hipStream_t stream) {
  (void)in_sizes; (void)n_in; (void)out_size; (void)ws_size;
  hipLaunchKernelGGL(fused_decode, dim3(NWG), dim3(NT), 0, stream, d_in[0], d_in[1], d_in[2], d_in[3], d_in[4], d_in[6], d_in[7], d_in[8], d_out, d_ws);
}
```

```cpp
#include <hip/hip_runtime.h>
#include <stdint.h>

namespace {
typedef unsigned u32;
typedef unsigned long long u64;
typedef u32 v4u __attribute__((ext_vector_type(4)));
typedef u32 v2u __attribute__((ext_vector_type(2)));
typedef float v2f __attribute__((ext_vector_type(2)));
typedef float v4f __attribute__((ext_vector_type(4)));
typedef __bf16 bf2 __attribute__((ext_vector_type(2)));
typedef __amdgpu_buffer_rsrc_t rsrc_t;

#define PACE_T 3950
#define PACE_LEAD (-60)
#define CATCH 50
#define WQ_PER 18
#define QPOLL_AT 8
#define GWE 72
#define GWB 10
#define GWB0 8
#define GW_PRE 0
#define GW_EARLY 2
#define CODE_BYTES 16128
#define VLAG 8
#define NREP 16

constexpr int D = 576, SK = 16384, DF = 4608;
constexpr int NWG = 256, NT = 576;
constexpr u32 KVB = 32u * SK * 128 * 2, WQB = D * 4096 * 2, WGB = D * DF * 2;
constexpr int SC1 = 16, NTL = 2;
constexpr u32 WS_EPOCH = 0;
constexpr u32 WS_ML = 4096;
constexpr u32 WS_Q = 65536;
constexpr u32 WS_P = WS_Q + 32 * 8 * 1024;
constexpr u32 WS_DN = WS_P + 72 * 256 * 64;
constexpr u32 HB_STRIDE = 2560;
constexpr u32 WS_HB = WS_DN + 72 * 144 * 64;
constexpr u32 WS_END = WS_HB + NREP * HB_STRIDE;
constexpr u32 OOB = 0x40000000u;
constexpr u32 SPIN_TICKS = 2000000u;
constexpr float L2E = 1.4426950408889634f;

__device__ __forceinline__ rsrc_t mk(const void* p, u32 bytes) { return __builtin_amdgcn_make_buffer_rsrc((void*)p, 0, bytes, 0x00020000); }
__device__ __forceinline__ float f32(u32 u) { return __builtin_bit_cast(float, u); }
__device__ __forceinline__ u32 bits(float f) { return __builtin_bit_cast(u32, f); }
__device__ __forceinline__ float blo(u32 u) { return f32(u << 16); }
__device__ __forceinline__ float bhi(u32 u) { return f32(u & 0xffff0000u); }
__device__ __forceinline__ float dot2(u32 a, u32 b, float c) { return __builtin_amdgcn_fdot2_f32_bf16(__builtin_bit_cast(bf2, a), __builtin_bit_cast(bf2, b), c, false); }
__device__ __forceinline__ u32 pk(float lo, float hi) { bf2 r = {(__bf16)lo, (__bf16)hi}; return __builtin_bit_cast(u32, r); }
__device__ __forceinline__ float rbf(float x) { return (float)(__bf16)x; }
__device__ __forceinline__ float ex2(float x) { return __builtin_amdgcn_exp2f(x); }
template <int CTRL> __device__ __forceinline__ float dpp(float x) { return f32(__builtin_amdgcn_mov_dpp(bits(x), CTRL, 0xf, 0xf, true)); }
template <int CTRL> __device__ __forceinline__ u32 dppu(u32 x) { return __builtin_amdgcn_mov_dpp(x, CTRL, 0xf, 0xf, true); }
constexpr int XOR1 = 0xB1, XOR2 = 0x4E, XOR7 = 0x141, XOR8 = 0x128, SHL4 = 0x104, BC0 = 0x150;
__device__ __forceinline__ float tr32(float a, float b) { auto s = __builtin_amdgcn_permlane32_swap(bits(a), bits(b), false, false); return f32(s[0]) + f32(s[1]); }
__device__ __forceinline__ float tr16(float a, float b) { auto s = __builtin_amdgcn_permlane16_swap(bits(a), bits(b), false, false); return f32(s[0]) + f32(s[1]); }
__device__ __forceinline__ float sum8(float x) { x += dpp<XOR1>(x); x += dpp<XOR2>(x); return x + dpp<XOR7>(x); }
__device__ __forceinline__ float max8(float x) { x = fmaxf(x, dpp<XOR1>(x)); x = fmaxf(x, dpp<XOR2>(x)); return fmaxf(x, dpp<XOR7>(x)); }
__device__ __forceinline__ float wave_sum(float x) { x = sum8(x); x += dpp<XOR8>(x); x = tr16(x, x); return tr32(x, x); }
__device__ __forceinline__ float wave_max(float x) {
  x = max8(x); x = fmaxf(x, dpp<XOR8>(x));
  auto s = __builtin_amdgcn_permlane16_swap(bits(x), bits(x), false, false); x = fmaxf(f32(s[0]), f32(s[1]));
  auto t = __builtin_amdgcn_permlane32_swap(bits(x), bits(x), false, false); return fmaxf(f32(t[0]), f32(t[1]));
}
__device__ __forceinline__ u32 plo(u32 a, u32 b) { return __builtin_amdgcn_perm(b, a, 0x05040100u); }
__device__ __forceinline__ u32 phi(u32 a, u32 b) { return __builtin_amdgcn_perm(b, a, 0x07060302u); }
__device__ __forceinline__ u32 now() { return (u32)__builtin_amdgcn_s_memrealtime(); }
#define SB() __builtin_amdgcn_sched_barrier(0)
#define CFENCE() asm volatile("" ::: "memory")
#define BAR() do { CFENCE(); __builtin_amdgcn_s_barrier(); CFENCE(); } while (0)
#define WG_BARRIER() do { asm volatile("s_waitcnt lgkmcnt(0)" ::: "memory"); __builtin_amdgcn_s_barrier(); CFENCE(); } while (0)
#define DRAIN() asm volatile("s_waitcnt vmcnt(0)" ::: "memory")
#define LD128(r, v, s, aux) __builtin_amdgcn_raw_buffer_load_b128(r, v, s, aux)
#define LD32(r, v, s, aux) __builtin_amdgcn_raw_buffer_load_b32(r, v, s, aux)
#define ST128(d, r, v, s, aux) __builtin_amdgcn_raw_buffer_store_b128(d, r, v, s, aux)
#define TAGOK(g) ((g).y == tag && (g).w == tag)
#define DMA1K_(pol, ldsaddr, vo, rs, soff) do { u32 keep_; \
    asm volatile("s_mov_b32 %0, m0\n\ts_mov_b32 m0, %1\n\ts_nop 0\n\tbuffer_load_dwordx4 %2, %3, %4 offen " pol "lds\n\ts_mov_b32 m0, %0" \
                 : "=&s"(keep_) : "s"(ldsaddr), "v"(vo), "s"(rs), "s"(soff) : "memory"); } while (0)
#define DMA1K(ldsaddr, vo, rs, soff) DMA1K_("nt ", ldsaddr, vo, rs, soff)
#define DMA4K(ldsaddr, vo, rs, soff) do { u32 keep_; \
    asm volatile("s_mov_b32 %0, m0\n\ts_mov_b32 m0, %1\n\ts_nop 0\n\tbuffer_load_dwordx4 %2, %3, %4 offen nt lds\n\t" \
                 "buffer_load_dwordx4 %2, %3, %4 offen offset:1024 nt lds\n\tbuffer_load_dwordx4 %2, %3, %4 offen offset:2048 nt lds\n\t" \
                 "buffer_load_dwordx4 %2, %3, %4 offen offset:3072 nt lds\n\ts_mov_b32 m0, %0" \
                 : "=&s"(keep_) : "s"(ldsaddr), "v"(vo), "s"(rs), "s"(soff) : "memory"); } while (0)

#define STAMP(i) do { } while (0)

constexpr int IMG_GW = 73728, STG_RED = 40960;

__global__ void __launch_bounds__(NT, 3) fused_decode(const void* __restrict__ px, const void* __restrict__ pk_, const void* __restrict__ pv,
    const void* __restrict__ pwq, const void* __restrict__ pwo, const void* __restrict__ pwg, const void* __restrict__ pwu,
    const void* __restrict__ pwd, void* pout, void* pws) {
  __shared__ __attribute__((aligned(16))) float lds[256 + 1024 + 640 + 256 + 36864];
  float* const qs = lds;
  float* const part = lds + 256;
  float* const misc = lds + 1280;
  float* const img = lds + 2176;
  const u32 img_lds = (u32)(uintptr_t)(__attribute__((address_space(3))) float*)img;
  u64 pc0;
  asm volatile("s_getpc_b64 %0" : "=s"(pc0));
  const u32 t0 = now();
  const int b = blockIdx.x, tid = threadIdx.x, lane = tid & 63, w = __builtin_amdgcn_readfirstlane(tid >> 6);
  const int r = lane >> 4, c = lane & 15, g16 = lane >> 2;
  const int xcd = b & 7, j = b >> 3, h = xcd * 4 + (j >> 3), sp = j & 7;
  const bool is_mlp = j < 18;
  const int cidx = xcd * 18 + j;
  const rsrc_t rx = mk(px, D * 2), rw = mk(pws, WS_END);
  const u32 voff = lane * 16;
  const u32 tp = t0 - PACE_LEAD;
#define SCHED(p) ((int)((u32)(p) * (u32)PACE_T >> 6))
#define DUE(p) do { const int due_ = SCHED(p); while ((int)(now() - tp) < due_) __builtin_amdgcn_s_sleep(1); } while (0)
  u32 tag;
#define READ_TAG() asm volatile("s_load_dword %0, %1, 0x0\n\ts_waitcnt lgkmcnt(0)\n\ts_add_u32 %0, %0, 1" : "=&s"(tag) : "s"(pws) : "scc", "memory")

  if (w == 8) {
    __builtin_amdgcn_s_setprio(3);
    const rsrc_t rq = mk(pwq, WQB), ro = mk(pwo, WQB), rgt = mk(pwg, WGB), rup = mk(pwu, WGB), rd = mk(pwd, WGB);
    const u32 gvo = g16 * 9216 + (lane & 3) * 16;
    u32 gw_n = is_mlp ? 0u : 72u;
#define GW_ONE(lim) do { if (gw_n < (u32)(lim)) { const bool up_ = gw_n >= 36; const rsrc_t rs_ = up_ ? rup : rgt; \
      DMA1K(img_lds + IMG_GW + gw_n * 1024, gvo, rs_, (up_ ? gw_n - 36 : gw_n) * (16 * 9216) + cidx * 64); ++gw_n; } } while (0)
    {
      const v4u xs0 = LD128(rx, lane * 32, 0, 0), xs1 = LD128(rx, lane * 32 + 16, 0, 0);
      const v4u xr0 = LD128(rx, 36 * r, 144 * sp, 0), xr1 = LD128(rx, 36 * r + 16, 144 * sp, 0);
      const u32 xr2 = LD32(rx, 36 * r + 32, 144 * sp, 0);
      if (CODE_BYTES) { const rsrc_t rcode = mk((const void*)(pc0 & ~255ull), CODE_BYTES); DMA1K_("", img_lds - 1024, voff, rcode, j * 1024); }
      v4u wq[18];
#pragma unroll
      for (int i = 0; i < 18; ++i) {
        if (i && i % WQ_PER == 0) DUE(i / WQ_PER);
        wq[i] = LD128(rq, r * (18 * 8192) + c * 16, (72 * sp + i) * 8192 + h * 256, NTL);
        SB();
      }
      STAMP(1);
      READ_TAG();
      const u32 ones = 0x3f803f80u;
      float s = 0.f;
#pragma unroll
      for (int d = 0; d < 4; ++d) { s = dot2(xs0[d], ones, s); s = dot2(xs1[d], ones, s); }
      const float mean = wave_sum(s) * (1.0f / D);
      float vs = 0.f;
#pragma unroll
      for (int d = 0; d < 4; ++d) {
        const float e0 = blo(xs0[d]) - mean, e1 = bhi(xs0[d]) - mean, e2 = blo(xs1[d]) - mean, e3 = bhi(xs1[d]) - mean;
        vs += e0 * e0 + e1 * e1 + e2 * e2 + e3 * e3;
      }
      vs = lane < 36 ? vs : 0.f;
      const float var = wave_sum(vs) * (1.0f / D);
      const float mb = rbf(mean), sd = rbf(__builtin_amdgcn_sqrtf(rbf(rbf(var) + 1e-5f)));
      const float rs = __builtin_amdgcn_rcpf(sd);
      float acc[8];
#pragma unroll
      for (int d = 0; d < 8; ++d) acc[d] = 0.f;
#pragma unroll
      for (int i = 0; i < 9; ++i) {
        const u32 xd = i < 4 ? xr0[i] : i < 8 ? xr1[i - 4] : xr2;
        const u32 xn = pk(rbf(blo(xd) - mb) * rs, rbf(bhi(xd) - mb) * rs);
#pragma unroll
        for (int d = 0; d < 4; ++d) {
          acc[2 * d] = dot2(plo(wq[2 * i][d], wq[2 * i + 1][d]), xn, acc[2 * d]);
          acc[2 * d + 1] = dot2(phi(wq[2 * i][d], wq[2 * i + 1][d]), xn, acc[2 * d + 1]);
        }
      }
      float t[4];
#pragma unroll
      for (int d = 0; d < 4; ++d) t[d] = tr32(acc[d], acc[d + 4]);
      ST128((v4u{bits(tr16(t[0], t[2])), tag, bits(tr16(t[1], t[3])), tag}), rw, voff, WS_Q + (h * 8 + sp) * 1024, SC1);
      STAMP(7);
#pragma unroll 1
      for (int k = 0; k < GWB0; ++k) GW_ONE(GWE);
      DUE(QPOLL_AT);
      const u32 tq = now();
      float qx, qy;
      for (;;) {
        CFENCE();
        v4u g[8];
#pragma unroll
        for (int i = 0; i < 8; ++i) g[i] = LD128(rw, voff, WS_Q + (h * 8 + i) * 1024, SC1);
        bool ok = true; qx = 0.f; qy = 0.f;
#pragma unroll
        for (int i = 0; i < 8; ++i) { ok = ok && TAGOK(g[i]); qx += f32(g[i].x); qy += f32(g[i].z); }
        if (__all(ok) || now() - tq > SPIN_TICKS) break;
        __builtin_amdgcn_s_sleep(16);
      }
      ((u32*)qs)[4 * c + r] = pk(qx, qy);
      STAMP(13);
#pragma unroll 1
      for (int k = 0; k < GWB0; ++k) { if (k == 0) DUE(13); GW_ONE(GWE); }
    }
    WG_BARRIER();
    u32 wo_src = h * (128 * 1152), wo_dst = img_lds;
    int prev = 0, p5 = 1;
    float mg = 0.f;
    v4u ml = {0u, 0u, 0u, 0u};
#pragma unroll 1
    for (int p = 16; p < 64; ++p) {
      const bool quiet = p == 38 || p == 39 || p >= 62;
      if (p == 39 || p == 63) DRAIN();
      { const int s_ = SCHED(p), m_ = prev + PACE_T * CATCH / 6400, due_ = s_ > m_ ? s_ : m_;
        while ((int)(now() - tp) < due_) __builtin_amdgcn_s_sleep(1);
        if (GW_PRE && !quiet && p5 == 5) for (int k = 0; k < GWB; ++k) GW_ONE(GWE);
        BAR();
        const int t_ = (int)(now() - tp); prev = t_ - due_ < 8 ? due_ : t_; }
      if (p == 40) {
        const v2f v = *(const v2f*)(qs + 64 + 2 * (lane & 7));
        mg = max8(v.x);
        const float lg = sum8(v.y * ex2((v.x - mg) * L2E));
        ST128((v4u{bits(mg), tag, bits(lg), tag}), rw, lane == 0 ? 0u : OOB, WS_ML + h * 256 + sp * 16, SC1);
        wo_dst = img_lds; p5 = 1;
      }
      if (!quiet) {
        if (p5 != 5) { DMA4K(wo_dst, voff, ro, wo_src); wo_dst += 4096; wo_src += 4096; ++p5; }
        else { if (!GW_PRE) for (int k = 0; k < GWB; ++k) GW_ONE(GWE); p5 = 1; }
      }
      if (p == 59) ml = LD128(rw, (lane & 7) * 16, WS_ML + h * 256, SC1);
      if (p == 62) {
        if (!__all(TAGOK(ml))) {
          const u32 tw = now();
          do { __builtin_amdgcn_s_sleep(8); CFENCE(); ml = LD128(rw, (lane & 7) * 16, WS_ML + h * 256, SC1); } while (!__all(TAGOK(ml)) && now() - tw < SPIN_TICKS);
        }
        const float M = max8(f32(ml.x));
        const float L = sum8(f32(ml.z) * ex2((f32(ml.x) - M) * L2E));
        if (lane == 0) qs[80] = ex2((mg - M) * L2E) * __builtin_amdgcn_rcpf(L);
      }
    }
    WG_BARRIER();
    BAR();
    if (is_mlp) {
      BAR();
      while (gw_n < 72) GW_ONE(72);
#pragma unroll 1
      for (int n = 0; n < 9; ++n) DMA4K(img_lds + n * 4096, voff, rd, cidx * (32 * 1152) + n * 4096);
      asm volatile("s_waitcnt vmcnt(36)" ::: "memory");
      BAR();
      DRAIN();
      BAR();
    }
  } else {
    const rsrc_t rk = mk(pk_, KVB), rv = mk(pv, KVB);
    const u32 kvbase = (u32)(h * 8 + sp) * 524288u + w * 1024;
    const char* const stg = (const char*)img + w * 9216;
    v4u a0[16], a[16];
#pragma unroll
    for (int i = 0; i < 32; ++i) {
      if (i && i % 2 == 0) DUE(i / 2);
      if (i < 16) a0[i & 15] = LD128(rk, voff, kvbase + i * 8192, NTL); else a[i & 15] = LD128(rk, voff, kvbase + i * 8192, NTL);
      SB();
    }
    READ_TAG();
    BAR();
    const v4u qv = *(const v4u*)(qs + 4 * c);
    STAMP(2);

#define TR_STEP(HALF, CTRL, BIT) _Pragma("unroll") for (int k = 0; k < (HALF); ++k) { \
      const float lo_ = x[k], hi_ = x[k + (HALF)]; const float t1_ = lo_ + dpp<CTRL>(lo_), t2_ = hi_ + dpp<CTRL>(hi_); x[k] = (c & (BIT)) ? t2_ : t1_; }
#define SCORE() TR_STEP(8, XOR8, 8) TR_STEP(4, XOR7, 4) TR_STEP(2, XOR2, 2) TR_STEP(1, XOR1, 1) \
      s0 = s1; s1 = s2; s2 = s3; s3 = rbf(rbf(x[0]) * 0.08838834764831845f);
#define QK(v) dot2((v)[3], qv.w, dot2((v)[2], qv.z, dot2((v)[1], qv.y, dot2((v)[0], qv.x, 0.f))))
    float s0 = 0.f, s1 = 0.f, s2 = 0.f, s3 = 0.f;
    {
      float x[16];
#pragma unroll
      for (int i = 0; i < 16; ++i) x[i] = QK(a0[i]);
      SCORE()
      SB();
    }
#pragma unroll 1
    for (int g = 1; g < 4; ++g) {
      const rsrc_t rn = g == 3 ? rv : rk;
      const u32 nb = kvbase + (g == 3 ? 0 : (g + 1) * 131072);
      float x[16];
#pragma unroll
      for (int i = 0; i < 16; i += 2) {
        x[i] = QK(a[i]); x[i + 1] = QK(a[i + 1]);
        asm volatile("" : "+v"(x[i]), "+v"(x[i + 1]));
        BAR();
        a[i] = LD128(rn, voff, nb + i * 8192, NTL); a[i + 1] = LD128(rn, voff, nb + (i + 1) * 8192, NTL);
        SB();
      }
      SCORE()
    }
    STAMP(3);
    const float mw = wave_max(fmaxf(fmaxf(s0, s1), fmaxf(s2, s3)));
    s0 = ex2((s0 - mw) * L2E); s1 = ex2((s1 - mw) * L2E); s2 = ex2((s2 - mw) * L2E); s3 = ex2((s3 - mw) * L2E);
    const float lw = wave_sum((s0 + s1) + (s2 + s3));
    if (lane == 0) *(v2f*)(qs + 64 + 2 * w) = v2f{mw, lw};
    s0 = rbf(s0); s1 = rbf(s1); s2 = rbf(s2); s3 = rbf(s3);
    u32 e[8][8], ex[4][2];
#define WO_READ(hf) do { _Pragma("unroll") for (int k = 0; k < 4; ++k) { \
      const v4u w0 = *(const v4u*)(stg + (2 * k) * 1152 + lane * 16), w1 = *(const v4u*)(stg + (2 * k + 1) * 1152 + lane * 16); \
      u32* e_ = e[4 * (hf) + k]; \
      _Pragma("unroll") for (int d = 0; d < 4; ++d) { e_[2 * d] = plo(w0[d], w1[d]); e_[2 * d + 1] = phi(w0[d], w1[d]); } \
      asm volatile("" : "+v"(e_[0]), "+v"(e_[1]), "+v"(e_[2]), "+v"(e_[3]), "+v"(e_[4]), "+v"(e_[5]), "+v"(e_[6]), "+v"(e_[7])); } \
    _Pragma("unroll") for (int k = 0; k < 2; ++k) { \
      const char* wp = stg + ((lane >> 5) * 4 + 2 * k) * 1152 + 1024 + (lane & 31) * 4; \
      const u32 w0 = *(const u32*)wp, w1 = *(const u32*)(wp + 1152); \
      ex[2 * (hf) + k][0] = plo(w0, w1); ex[2 * (hf) + k][1] = phi(w0, w1); \
      asm volatile("" : "+v"(ex[2 * (hf) + k][0]), "+v"(ex[2 * (hf) + k][1])); } \
    asm volatile("s_waitcnt lgkmcnt(0)" ::: "memory"); SB(); } while (0)
    WO_READ(0);

    float acc[8];
#pragma unroll
    for (int d = 0; d < 8; ++d) acc[d] = 0.f;
#define PV(i, sv) do { const u32 ph_ = dppu<BC0 + (i)>(bits(sv)), pl_ = ph_ >> 16; _Pragma("unroll") for (int d = 0; d < 4; ++d) { \
      acc[2 * d] = dot2(a[i][d], pl_, acc[2 * d]); acc[2 * d + 1] = dot2(a[i][d], ph_, acc[2 * d + 1]); } \
    asm volatile("" : "+v"(acc[0]), "+v"(acc[1]), "+v"(acc[2]), "+v"(acc[3]), "+v"(acc[4]), "+v"(acc[5]), "+v"(acc[6]), "+v"(acc[7])); } while (0)
#define REP8(M) M(0) M(2) M(4) M(6) M(8) M(10) M(12) M(14)
#define PVL(i) do { if ((i) + 16 - VLAG < 16) PV(((i) + 16 - VLAG) & 15, s0); else PV(((i) + 16 - VLAG) & 15, s1); } while (0)
#define VHEAD(i) if ((i) < 16 - VLAG) { PV(i, s0); PV((i) + 1, s0); SB(); }
    REP8(VHEAD)
#undef VHEAD
#define VSTEP(i) PVL(i); PVL((i) + 1); BAR(); a[i] = LD128(rv, voff, nb + (i) * 8192, NTL); a[(i) + 1] = LD128(rv, voff, nb + ((i) + 1) * 8192, NTL); SB();
#pragma unroll 1
    for (int g = 0; g < 3; ++g) {
      const u32 nb = kvbase + (g + 1) * 131072;
      REP8(VSTEP)
      s0 = s1; s1 = s2; s2 = s3;
    }
#undef VSTEP
    STAMP(4);
    WO_READ(1);
    const float scale = ex2((mw - max8(qs[64 + 2 * (lane & 7)])) * L2E);
#define VLAST(i) if ((i) >= 16 - VLAG) { PV(i, s0); SB(); PV((i) + 1, s0); SB(); }
    REP8(VLAST)
#undef VLAST
    STAMP(5);

    {
      float* const red = img + STG_RED / 4;
      float t[4];
#pragma unroll
      for (int d = 0; d < 4; ++d) t[d] = tr32(acc[d], acc[d + 4]);
      *(v2f*)(part + w * 128 + 8 * c + 2 * r) = v2f{tr16(t[0], t[2]) * scale, tr16(t[1], t[3]) * scale};
      WG_BARRIER();
      v2f o = *(const v2f*)(part + (lane >> 3) * 128 + (lane & 4) * 16 + 8 * w + 2 * (lane & 3));
      const float F = qs[80];
      o.x += dpp<XOR8>(o.x); o.y += dpp<XOR8>(o.y);
      o.x = tr16(o.x, o.x); o.y = tr16(o.y, o.y); o.x = tr32(o.x, o.x); o.y = tr32(o.y, o.y);
      const u32 op = pk(o.x * F, o.y * F);
      float pm[8], px0 = 0.f, px1 = 0.f;
#pragma unroll
      for (int n = 0; n < 8; ++n) pm[n] = 0.f;
#define OSTEP(k) { const u32 ok_ = dppu<BC0 + (k)>(op); _Pragma("unroll") for (int n = 0; n < 8; ++n) pm[n] = dot2(e[k][n], ok_, pm[n]); }
      OSTEP(0) OSTEP(1) OSTEP(2) OSTEP(3) OSTEP(4) OSTEP(5) OSTEP(6) OSTEP(7)
#undef OSTEP
#define XSTEP(k) { u32 ok_ = __builtin_amdgcn_update_dpp(0u, op, BC0 + ((k) >> 1) * 4 + ((k) & 1), 0x3, 0xf, false); \
        ok_ = __builtin_amdgcn_update_dpp(ok_, op, BC0 + ((k) >> 1) * 4 + 2 + ((k) & 1), 0xc, 0xf, false); \
        px0 = dot2(ex[k][0], ok_, px0); px1 = dot2(ex[k][1], ok_, px1); }
      XSTEP(0) XSTEP(1) XSTEP(2) XSTEP(3)
#undef XSTEP
      const float pxs = tr32(px0, px1);
      float* rw_ = red + w * 640;
      *(v4f*)(rw_ + lane * 4) = v4f{pm[0], pm[1], pm[2], pm[3]};
      *(v4f*)(rw_ + 288 + lane * 4) = v4f{pm[4], pm[5], pm[6], pm[7]};
      rw_[576 + 2 * (lane & 31) + (lane >> 5)] = pxs;
      WG_BARRIER();
      if (w < 5) {
        const int off = tid < 256 ? ((tid >> 1) & 1) * 288 + (tid >> 2) * 4 + 2 * (tid & 1) : 576 + 2 * (tid & 31);
        v2f s = {0.f, 0.f};
#pragma unroll
        for (int i = 0; i < 8; ++i) s += *(const v2f*)(red + i * 640 + off);
        ST128((v4u{bits(s.x), tag, bits(s.y), tag}), rw, tid < 288 ? (tid >> 2) * (256 * 64) + (tid & 3) * 16 : OOB, WS_P + b * 64, SC1);
      }
    }
    STAMP(6);

    if (is_mlp) {
      BAR();
      BAR();
      u32 ge[4][8]; v4u g8;
      {
        const char* gl = (const char*)img + IMG_GW + w * 9216 + (lane & 3) * 16;
#pragma unroll
        for (int k = 0; k < 4; ++k) {
          const v4u w0 = *(const v4u*)(gl + (8 * g16 + 2 * k) * 64), w1 = *(const v4u*)(gl + (8 * g16 + 2 * k + 1) * 64);
#pragma unroll
          for (int d = 0; d < 4; ++d) { ge[k][2 * d] = plo(w0[d], w1[d]); ge[k][2 * d + 1] = phi(w0[d], w1[d]); }
        }
        g8 = *(const v4u*)(gl + (128 + g16) * 64);
      }
      const u32 hso = WS_HB + (u32)(cidx % NREP) * HB_STRIDE + 576 * (w & 3);
      u32 dw[32];
#pragma unroll
      for (int k = 0; k < 32; ++k) dw[k] = 0u;
      float Sg = 0.f, Su = 0.f, Ag = 0.f, Au = 0.f, sh = 0.f, sq = 0.f;
#pragma unroll 1
      for (int it = 0; it < 2; ++it) {
        u32 hp0 = 0x3f803f80u, hp1 = hp0, hp2 = hp0, hp3 = hp0, h8 = 0x3f80u;
        if (it) {
          const char* dl = (const char*)img + (tid < 288 ? tid : 0) * 4;
#pragma unroll
          for (int k = 0; k < 16; ++k) { const char* dp = dl + k * 2304; const u32 w0 = *(const u32*)dp, w1 = *(const u32*)(dp + 1152);
            dw[2 * k] = plo(w0, w1); dw[2 * k + 1] = phi(w0, w1); asm volatile("" : "+v"(dw[2 * k]), "+v"(dw[2 * k + 1])); }
          STAMP(1);
          v4u ga, gb, gc;
          const u32 tw = now();
          for (;;) {
            CFENCE();
            ga = LD128(rw, g16 * 32, hso, SC1); gb = LD128(rw, g16 * 32 + 16, hso, SC1); gc = LD128(rw, 512 + (g16 >> 2) * 16, hso, SC1);
            if (__all(TAGOK(ga) && TAGOK(gb) && TAGOK(gc)) || now() - tw > SPIN_TICKS) break;
          }
          hp0 = ga.x; hp1 = ga.z; hp2 = gb.x; hp3 = gb.z;
          const u32 pr = (g16 & 2) ? gc.z : gc.x;
          h8 = (g16 & 1) ? pr >> 16 : pr & 0xffffu;
          STAMP(8);
        }
        float y[8];
#pragma unroll
        for (int n = 0; n < 8; ++n) {
          float s = dot2(ge[0][n], hp0, 0.f); s = dot2(ge[1][n], hp1, s); s = dot2(ge[2][n], hp2, s); s = dot2(ge[3][n], hp3, s);
          y[n] = dot2(g8[n >> 1], (n & 1) ? h8 << 16 : h8, s);
        }
        const u32 ones = 0x3f803f80u;
        float a1 = dot2(hp0, ones, dot2(hp1, ones, dot2(hp2, ones, dot2(hp3, ones, dot2(h8, ones, 0.f)))));
        float a2 = dot2(hp0, hp0, dot2(hp1, hp1, dot2(hp2, hp2, dot2(hp3, hp3, dot2(h8, h8, 0.f)))));
        float t[4];
#pragma unroll
        for (int d = 0; d < 4; ++d) t[d] = tr32(y[d], y[d + 4]);
        const float u0 = tr16(t[0], t[2]), u1 = tr16(t[1], t[3]);
        const float v0 = u0 + dpp<XOR8>(u0), v1 = u1 + dpp<XOR8>(u1);
        float z = (lane & 8) ? v1 : v0;
        z += dpp<SHL4>(z);
        float st = tr32(a1, a2); st = tr16(st, st); st += dpp<XOR8>(st); st += dpp<SHL4>(st);
        float* gp = misc + it * 256;
        if (!(lane & 4)) gp[w * 32 + (lane & 3) * 8 + (lane >> 3)] = z;
        if ((lane & 31) == 0) misc[512 + w * 2 + (lane >> 5)] = st;
        WG_BARRIER();
        const int n = lane & 31;
        Ag = (gp[n] + gp[32 + n]) + (gp[64 + n] + gp[96 + n]);
        Au = (gp[128 + n] + gp[160 + n]) + (gp[192 + n] + gp[224 + n]);
        if (it == 0) { Sg = Ag; Su = Au; }
        else { const v4f p0 = *(const v4f*)(misc + 512), p1 = *(const v4f*)(misc + 516); sh = (p0.x + p0.z) + (p1.x + p1.z); sq = (p0.y + p0.w) + (p1.y + p1.w); }
      }
      const float mean = sh * (1.0f / D), var = fmaxf(sq * (1.0f / D) - mean * mean, 0.f);
      const float mb = rbf(mean), rs = __builtin_amdgcn_rcpf(rbf(__builtin_amdgcn_sqrtf(rbf(rbf(var) + 1e-5f))));
      const float gv = rbf((Ag - mb * Sg) * rs), uv = rbf((Au - mb * Su) * rs);
      const float zz = gv * (1.0f + 0.044715f * gv * gv) * (-2.0f * 0.7978845608028654f * L2E);
      const float gl = rbf(gv * __builtin_amdgcn_rcpf(1.0f + ex2(zz)));
      const u32 ab = bits(rbf(gl * uv));
      const u32 ap = (ab >> 16) | dppu<XOR1>(ab);
      float d0 = 0.f, d1 = 0.f;
#pragma unroll
      for (int k = 0; k < 16; ++k) { const u32 s = __builtin_amdgcn_readlane(ap, 2 * k); d0 = dot2(dw[2 * k], s, d0); d1 = dot2(dw[2 * k + 1], s, d1); }
      ST128((v4u{bits(d0), tag, bits(d1), tag}), rw, tid < 288 ? (tid >> 2) * (144 * 64) + (tid & 3) * 16 : OOB, WS_DN + cidx * 64, SC1);
      STAMP(9);
    } else if (j < 27) {
      const int rr = xcd * 9 + (j - 18);
      const v2u xv = __builtin_amdgcn_raw_buffer_load_b64(rx, (lane & 1) * 8, rr * 16, 0);
      float res0 = blo(xv.x), res1 = bhi(xv.x), res2 = blo(xv.y), res3 = bhi(xv.y);
#pragma unroll 1
      for (int it = 0; it < 2; ++it) {
        const u32 so = it ? WS_DN + rr * (144 * 64) : WS_P + rr * (256 * 64);
        const bool has2 = !it || tid < 64;
        v4u ga = {0u, 0u, 0u, 0u}, gb = ga;
        bool oka = false, okb = !has2;
        const u32 tw = now();
        for (;;) {
          CFENCE();
          if (!oka) ga = LD128(rw, tid * 16, so, SC1);
          if (!okb) gb = LD128(rw, tid * 16 + 8192, so, SC1);
          oka = TAGOK(ga); okb = !has2 || TAGOK(gb);
          if (__all(oka && okb) || now() - tw > SPIN_TICKS) break;
        }
        if (it == 0) STAMP(10); else STAMP(12);
        const float c0 = f32(ga.x) + f32(gb.x), c1 = f32(ga.z) + f32(gb.z);
        float z = tr32(c0, c1); z = tr16(z, z); z += dpp<XOR8>(z); z += dpp<SHL4>(z);
        float* gp = misc + it * 64;
        if ((lane & 31) < 4) gp[w * 8 + 2 * (lane & 3) + (lane >> 5)] = z;
        WG_BARRIER();
        if (w == 0) {
          v4f s = {0.f, 0.f, 0.f, 0.f};
#pragma unroll
          for (int i = 0; i < 8; ++i) s += *(const v4f*)(gp + i * 8 + (lane & 1) * 4);
          res0 = rbf(res0 + rbf(s.x)); res1 = rbf(res1 + rbf(s.y)); res2 = rbf(res2 + rbf(s.z)); res3 = rbf(res3 + rbf(s.w));
          const u32 p0 = (bits(res0) >> 16) | bits(res1), p1 = (bits(res2) >> 16) | bits(res3);
          if (it == 0) {
            ST128((v4u{p0, tag, p1, tag}), rw, lane < 2 * NREP ? (lane >> 1) * HB_STRIDE + (lane & 1) * 16 : OOB, WS_HB + rr * 32, SC1);
          } else {
            const rsrc_t rout = mk(pout, D * 2);
            __builtin_amdgcn_raw_buffer_store_b64(v2u{p0, p1}, rout, lane < 2 ? lane * 8 : OOB, rr * 16, 0);
            if (rr == 0) __builtin_amdgcn_raw_buffer_store_b32(tag, rw, lane == 0 ? WS_EPOCH : OOB, 0, SC1);
          }
        }
        if (it == 0) STAMP(11);
      }
    }
  }
}
}

extern "C" void kernel_launch(void* const* d_in, const int* in_sizes, int n_in, void* d_out, int out_size, void* d_ws, size_t ws_size,
                              hipStream_t stream) {
  (void)in_sizes; (void)n_in; (void)out_size; (void)ws_size;
  hipLaunchKernelGGL(fused_decode, dim3(NWG), dim3(NT), 0, stream, d_in[0], d_in[1], d_in[2], d_in[3], d_in[4], d_in[6], d_in[7], d_in[8], d_out, d_ws);
}
```

```cpp
#define PROBE 0
#define PROBE_MUL 0
#define PROBE_SEL (b == 7 && w==0)
#include <hip/hip_runtime.h>
#include <stdint.h>

namespace {
typedef unsigned u32;
typedef unsigned long long u64;
typedef u32 v4u __attribute__((ext_vector_type(4)));
typedef u32 v2u __attribute__((ext_vector_type(2)));
typedef float v2f __attribute__((ext_vector_type(2)));
typedef float v4f __attribute__((ext_vector_type(4)));
typedef __bf16 bf2 __attribute__((ext_vector_type(2)));
typedef __amdgpu_buffer_rsrc_t rsrc_t;

#define PACE_T 3950
#define PACE_LEAD (-60)
#define CATCH 50
#define WQ_PER 18
#define QPOLL_AT 8
#define GWE 72
#define GWB 10
#define GWB0 8
#define GW_PRE 0
#define GW_EARLY 2
#define CODE_BYTES 16128
#define VLAG 8
#define NREP 16

constexpr int D = 576, SK = 16384, DF = 4608;
constexpr int NWG = 256, NT = 576;
constexpr u32 KVB = 32u * SK * 128 * 2, WQB = D * 4096 * 2, WGB = D * DF * 2;
constexpr int SC1 = 16, NTL = 2;
constexpr u32 WS_EPOCH = 0;
constexpr u32 WS_ML = 4096;
constexpr u32 WS_Q = 65536;
constexpr u32 WS_P = WS_Q + 32 * 8 * 1024;
constexpr u32 WS_DN = WS_P + 72 * 256 * 64;
constexpr u32 HB_STRIDE = 2560;
constexpr u32 WS_HB = WS_DN + 72 * 144 * 64;
constexpr u32 WS_END = WS_HB + NREP * HB_STRIDE;
constexpr u32 OOB = 0x40000000u;
constexpr u32 SPIN_TICKS = 2000000u;
constexpr float L2E = 1.4426950408889634f;

__device__ __forceinline__ rsrc_t mk(const void* p, u32 bytes) { return __builtin_amdgcn_make_buffer_rsrc((void*)p, 0, bytes, 0x00020000); }
__device__ __forceinline__ float f32(u32 u) { return __builtin_bit_cast(float, u); }
__device__ __forceinline__ u32 bits(float f) { return __builtin_bit_cast(u32, f); }
__device__ __forceinline__ float blo(u32 u) { return f32(u << 16); }
__device__ __forceinline__ float bhi(u32 u) { return f32(u & 0xffff0000u); }
__device__ __forceinline__ float dot2(u32 a, u32 b, float c) { return __builtin_amdgcn_fdot2_f32_bf16(__builtin_bit_cast(bf2, a), __builtin_bit_cast(bf2, b), c, false); }
__device__ __forceinline__ u32 pk(float lo, float hi) { bf2 r = {(__bf16)lo, (__bf16)hi}; return __builtin_bit_cast(u32, r); }
__device__ __forceinline__ float rbf(float x) { return (float)(__bf16)x; }
__device__ __forceinline__ float ex2(float x) { return __builtin_amdgcn_exp2f(x); }
template <int CTRL> __device__ __forceinline__ float dpp(float x) { return f32(__builtin_amdgcn_mov_dpp(bits(x), CTRL, 0xf, 0xf, true)); }
template <int CTRL> __device__ __forceinline__ u32 dppu(u32 x) { return __builtin_amdgcn_mov_dpp(x, CTRL, 0xf, 0xf, true); }
constexpr int XOR1 = 0xB1, XOR2 = 0x4E, XOR7 = 0x141, XOR8 = 0x128, SHL4 = 0x104, BC0 = 0x150;
__device__ __forceinline__ float tr32(float a, float b) { auto s = __builtin_amdgcn_permlane32_swap(bits(a), bits(b), false, false); return f32(s[0]) + f32(s[1]); }
__device__ __forceinline__ float tr16(float a, float b) { auto s = __builtin_amdgcn_permlane16_swap(bits(a), bits(b), false, false); return f32(s[0]) + f32(s[1]); }
__device__ __forceinline__ float sum8(float x) { x += dpp<XOR1>(x); x += dpp<XOR2>(x); return x + dpp<XOR7>(x); }
__device__ __forceinline__ float max8(float x) { x = fmaxf(x, dpp<XOR1>(x)); x = fmaxf(x, dpp<XOR2>(x)); return fmaxf(x, dpp<XOR7>(x)); }
__device__ __forceinline__ float wave_sum(float x) { x = sum8(x); x += dpp<XOR8>(x); x = tr16(x, x); return tr32(x, x); }
__device__ __forceinline__ float wave_max(float x) {
  x = max8(x); x = fmaxf(x, dpp<XOR8>(x));
  auto s = __builtin_amdgcn_permlane16_swap(bits(x), bits(x), false, false); x = fmaxf(f32(s[0]), f32(s[1]));
  auto t = __builtin_amdgcn_permlane32_swap(bits(x), bits(x), false, false); return fmaxf(f32(t[0]), f32(t[1]));
}
__device__ __forceinline__ u32 plo(u32 a, u32 b) { return __builtin_amdgcn_perm(b, a, 0x05040100u); }
__device__ __forceinline__ u32 phi(u32 a, u32 b) { return __builtin_amdgcn_perm(b, a, 0x07060302u); }
__device__ __forceinline__ u32 now() { return (u32)__builtin_amdgcn_s_memrealtime(); }
#define SB() __builtin_amdgcn_sched_barrier(0)
#define CFENCE() asm volatile("" ::: "memory")
#define BAR() do { CFENCE(); __builtin_amdgcn_s_barrier(); CFENCE(); } while (0)
#define WG_BARRIER() do { asm volatile("s_waitcnt lgkmcnt(0)" ::: "memory"); __builtin_amdgcn_s_barrier(); CFENCE(); } while (0)
#define DRAIN() asm volatile("s_waitcnt vmcnt(0)" ::: "memory")
#define LD128(r, v, s, aux) __builtin_amdgcn_raw_buffer_load_b128(r, v, s, aux)
#define LD32(r, v, s, aux) __builtin_amdgcn_raw_buffer_load_b32(r, v, s, aux)
#define ST128(d, r, v, s, aux) __builtin_amdgcn_raw_buffer_store_b128(d, r, v, s, aux)
#define TAGOK(g) ((g).y == tag && (g).w == tag)
#define DMA1K_(pol, ldsaddr, vo, rs, soff) do { u32 keep_; \
    asm volatile("s_mov_b32 %0, m0\n\ts_mov_b32 m0, %1\n\ts_nop 0\n\tbuffer_load_dwordx4 %2, %3, %4 offen " pol "lds\n\ts_mov_b32 m0, %0" \
                 : "=&s"(keep_) : "s"(ldsaddr), "v"(vo), "s"(rs), "s"(soff) : "memory"); } while (0)
#define DMA1K(ldsaddr, vo, rs, soff) DMA1K_("nt ", ldsaddr, vo, rs, soff)
#define DMA4K(ldsaddr, vo, rs, soff) do { u32 keep_; \
    asm volatile("s_mov_b32 %0, m0\n\ts_mov_b32 m0, %1\n\ts_nop 0\n\tbuffer_load_dwordx4 %2, %3, %4 offen nt lds\n\t" \
                 "buffer_load_dwordx4 %2, %3, %4 offen offset:1024 nt lds\n\tbuffer_load_dwordx4 %2, %3, %4 offen offset:2048 nt lds\n\t" \
                 "buffer_load_dwordx4 %2, %3, %4 offen offset:3072 nt lds\n\ts_mov_b32 m0, %0" \
                 : "=&s"(keep_) : "s"(ldsaddr), "v"(vo), "s"(rs), "s"(soff) : "memory"); } while (0)

#define NSTAMP 16
#define STAMP(i) do { SB(); ts[i] = now(); SB(); } while (0)

constexpr int IMG_GW = 73728, STG_RED = 40960;

__global__ void __launch_bounds__(NT, 3) fused_decode(const void* __restrict__ px, const void* __restrict__ pk_, const void* __restrict__ pv,
    const void* __restrict__ pwq, const void* __restrict__ pwo, const void* __restrict__ pwg, const void* __restrict__ pwu,
    const void* __restrict__ pwd, void* pout, void* pws) {
  __shared__ __attribute__((aligned(16))) float lds[256 + 1024 + 640 + 256 + 36864];
  float* const qs = lds;
  float* const part = lds + 256;
  float* const misc = lds + 1280;
  float* const img = lds + 2176;
  const u32 img_lds = (u32)(uintptr_t)(__attribute__((address_space(3))) float*)img;
  u64 pc0;
  asm volatile("s_getpc_b64 %0" : "=s"(pc0));
  const u32 t0 = now();
  u32 ts[NSTAMP];
#pragma unroll
  for (int i = 0; i < NSTAMP; ++i) ts[i] = 0;
  ts[0] = t0;
  const int b = blockIdx.x, tid = threadIdx.x, lane = tid & 63, w = __builtin_amdgcn_readfirstlane(tid >> 6);
  const int r = lane >> 4, c = lane & 15, g16 = lane >> 2;
  const int xcd = b & 7, j = b >> 3, h = xcd * 4 + (j >> 3), sp = j & 7;
  const bool is_mlp = j < 18;
  const int cidx = xcd * 18 + j;
  const rsrc_t rx = mk(px, D * 2), rw = mk(pws, WS_END);
  const u32 voff = lane * 16;
  const u32 tp = t0 - PACE_LEAD;
#define SCHED(p) ((int)((u32)(p) * (u32)PACE_T >> 6))
#define DUE(p) do { const int due_ = SCHED(p); while ((int)(now() - tp) < due_) __builtin_amdgcn_s_sleep(1); } while (0)
  u32 tag;
#define READ_TAG() asm volatile("s_load_dword %0, %1, 0x0\n\ts_waitcnt lgkmcnt(0)\n\ts_add_u32 %0, %0, 1" : "=&s"(tag) : "s"(pws) : "scc", "memory")

  if (w == 8) {
    __builtin_amdgcn_s_setprio(3);
    const rsrc_t rq = mk(pwq, WQB), ro = mk(pwo, WQB), rgt = mk(pwg, WGB), rup = mk(pwu, WGB), rd = mk(pwd, WGB);
    const u32 gvo = g16 * 9216 + (lane & 3) * 16;
    u32 gw_n = is_mlp ? 0u : 72u;
#define GW_ONE(lim) do { if (gw_n < (u32)(lim)) { const bool up_ = gw_n >= 36; const rsrc_t rs_ = up_ ? rup : rgt; \
      DMA1K(img_lds + IMG_GW + gw_n * 1024, gvo, rs_, (up_ ? gw_n - 36 : gw_n) * (16 * 9216) + cidx * 64); ++gw_n; } } while (0)
    {
      const v4u xs0 = LD128(rx, lane * 32, 0, 0), xs1 = LD128(rx, lane * 32 + 16, 0, 0);
      const v4u xr0 = LD128(rx, 36 * r, 144 * sp, 0), xr1 = LD128(rx, 36 * r + 16, 144 * sp, 0);
      const u32 xr2 = LD32(rx, 36 * r + 32, 144 * sp, 0);
      if (CODE_BYTES) { const rsrc_t rcode = mk((const void*)(pc0 & ~255ull), CODE_BYTES); DMA1K_("", img_lds - 1024, voff, rcode, j * 1024); }
      v4u wq[18];
#pragma unroll
      for (int i = 0; i < 18; ++i) {
        if (i && i % WQ_PER == 0) DUE(i / WQ_PER);
        wq[i] = LD128(rq, r * (18 * 8192) + c * 16, (72 * sp + i) * 8192 + h * 256, NTL);
        SB();
      }
      STAMP(1);
      READ_TAG();
      const u32 ones = 0x3f803f80u;
      float s = 0.f;
#pragma unroll
      for (int d = 0; d < 4; ++d) { s = dot2(xs0[d], ones, s); s = dot2(xs1[d], ones, s); }
      const float mean = wave_sum(s) * (1.0f / D);
      float vs = 0.f;
#pragma unroll
      for (int d = 0; d < 4; ++d) {
        const float e0 = blo(xs0[d]) - mean, e1 = bhi(xs0[d]) - mean, e2 = blo(xs1[d]) - mean, e3 = bhi(xs1[d]) - mean;
        vs += e0 * e0 + e1 * e1 + e2 * e2 + e3 * e3;
      }
      vs = lane < 36 ? vs : 0.f;
      const float var = wave_sum(vs) * (1.0f / D);
      const float mb = rbf(mean), sd = rbf(__builtin_amdgcn_sqrtf(rbf(rbf(var) + 1e-5f)));
      const float rs = __builtin_amdgcn_rcpf(sd);
      float acc[8];
#pragma unroll
      for (int d = 0; d < 8; ++d) acc[d] = 0.f;
#pragma unroll
      for (int i = 0; i < 9; ++i) {
        const u32 xd = i < 4 ? xr0[i] : i < 8 ? xr1[i - 4] : xr2;
        const u32 xn = pk(rbf(blo(xd) - mb) * rs, rbf(bhi(xd) - mb) * rs);
#pragma unroll
        for (int d = 0; d < 4; ++d) {
          acc[2 * d] = dot2(plo(wq[2 * i][d], wq[2 * i + 1][d]), xn, acc[2 * d]);
          acc[2 * d + 1] = dot2(phi(wq[2 * i][d], wq[2 * i + 1][d]), xn, acc[2 * d + 1]);
        }
      }
      float t[4];
#pragma unroll
      for (int d = 0; d < 4; ++d) t[d] = tr32(acc[d], acc[d + 4]);
      ST128((v4u{bits(tr16(t[0], t[2])), tag, bits(tr16(t[1], t[3])), tag}), rw, voff, WS_Q + (h * 8 + sp) * 1024, SC1);
      STAMP(7);
#pragma unroll 1
      for (int k = 0; k < GWB0; ++k) GW_ONE(GWE);
      DUE(QPOLL_AT);
      const u32 tq = now();
      float qx, qy;
      for (;;) {
        CFENCE();
        v4u g[8];
#pragma unroll
        for (int i = 0; i < 8; ++i) g[i] = LD128(rw, voff, WS_Q + (h * 8 + i) * 1024, SC1);
        bool ok = true; qx = 0.f; qy = 0.f;
#pragma unroll
        for (int i = 0; i < 8; ++i) { ok = ok && TAGOK(g[i]); qx += f32(g[i].x); qy += f32(g[i].z); }
        if (__all(ok) || now() - tq > SPIN_TICKS) break;
        __builtin_amdgcn_s_sleep(16);
      }
      ((u32*)qs)[4 * c + r] = pk(qx, qy);
      STAMP(13);
#pragma unroll 1
      for (int k = 0; k < GWB0; ++k) { if (k == 0) DUE(13); GW_ONE(GWE); }
    }
    WG_BARRIER();
    u32 wo_src = h * (128 * 1152), wo_dst = img_lds;
    int prev = 0, p5 = 1;
    float mg = 0.f;
    v4u ml = {0u, 0u, 0u, 0u};
#pragma unroll 1
    for (int p = 16; p < 64; ++p) {
      const bool quiet = p == 38 || p == 39 || p >= 62;
      if (p == 39 || p == 63) DRAIN();
      { const int s_ = SCHED(p), m_ = prev + PACE_T * CATCH / 6400, due_ = s_ > m_ ? s_ : m_;
        while ((int)(now() - tp) < due_) __builtin_amdgcn_s_sleep(1);
        if (GW_PRE && !quiet && p5 == 5) for (int k = 0; k < GWB; ++k) GW_ONE(GWE);
        BAR();
        const int t_ = (int)(now() - tp); prev = t_ - due_ < 8 ? due_ : t_; }
      if (p == 40) {
        const v2f v = *(const v2f*)(qs + 64 + 2 * (lane & 7));
        mg = max8(v.x);
        const float lg = sum8(v.y * ex2((v.x - mg) * L2E));
        ST128((v4u{bits(mg), tag, bits(lg), tag}), rw, lane == 0 ? 0u : OOB, WS_ML + h * 256 + sp * 16, SC1);
        wo_dst = img_lds; p5 = 1;
      }
      if (!quiet) {
        if (p5 != 5) { DMA4K(wo_dst, voff, ro, wo_src); wo_dst += 4096; wo_src += 4096; ++p5; }
        else { if (!GW_PRE) for (int k = 0; k < GWB; ++k) GW_ONE(GWE); p5 = 1; }
      }
      if (p == 59) ml = LD128(rw, (lane & 7) * 16, WS_ML + h * 256, SC1);
      if (p == 62) {
        if (!__all(TAGOK(ml))) {
          const u32 tw = now();
          do { __builtin_amdgcn_s_sleep(8); CFENCE(); ml = LD128(rw, (lane & 7) * 16, WS_ML + h * 256, SC1); } while (!__all(TAGOK(ml)) && now() - tw < SPIN_TICKS);
        }
        const float M = max8(f32(ml.x));
        const float L = sum8(f32(ml.z) * ex2((f32(ml.x) - M) * L2E));
        if (lane == 0) qs[80] = ex2((mg - M) * L2E) * __builtin_amdgcn_rcpf(L);
      }
    }
    WG_BARRIER();
    BAR();
    if (is_mlp) {
      BAR();
      while (gw_n < 72) GW_ONE(72);
#pragma unroll 1
      for (int n = 0; n < 9; ++n) DMA4K(img_lds + n * 4096, voff, rd, cidx * (32 * 1152) + n * 4096);
      asm volatile("s_waitcnt vmcnt(36)" ::: "memory");
      BAR();
      DRAIN();
      BAR();
    }
  } else {
    const rsrc_t rk = mk(pk_, KVB), rv = mk(pv, KVB);
    const u32 kvbase = (u32)(h * 8 + sp) * 524288u + w * 1024;
    const char* const stg = (const char*)img + w * 9216;
    v4u a0[16], a[16];
#pragma unroll
    for (int i = 0; i < 32; ++i) {
      if (i && i % 2 == 0) DUE(i / 2);
      if (i < 16) a0[i & 15] = LD128(rk, voff, kvbase + i * 8192, NTL); else a[i & 15] = LD128(rk, voff, kvbase + i * 8192, NTL);
      SB();
    }
    READ_TAG();
    BAR();
    const v4u qv = *(const v4u*)(qs + 4 * c);
    STAMP(2);

#define TR_STEP(HALF, CTRL, BIT) _Pragma("unroll") for (int k = 0; k < (HALF); ++k) { \
      const float lo_ = x[k], hi_ = x[k + (HALF)]; const float t1_ = lo_ + dpp<CTRL>(lo_), t2_ = hi_ + dpp<CTRL>(hi_); x[k] = (c & (BIT)) ? t2_ : t1_; }
#define SCORE() TR_STEP(8, XOR8, 8) TR_STEP(4, XOR7, 4) TR_STEP(2, XOR2, 2) TR_STEP(1, XOR1, 1) \
      s0 = s1; s1 = s2; s2 = s3; s3 = rbf(rbf(x[0]) * 0.08838834764831845f);
#define QK(v) dot2((v)[3], qv.w, dot2((v)[2], qv.z, dot2((v)[1], qv.y, dot2((v)[0], qv.x, 0.f))))
    float s0 = 0.f, s1 = 0.f, s2 = 0.f, s3 = 0.f;
    {
      float x[16];
#pragma unroll
      for (int i = 0; i < 16; ++i) x[i] = QK(a0[i]);
      SCORE()
      SB();
    }
#pragma unroll 1
    for (int g = 1; g < 4; ++g) {
      const rsrc_t rn = g == 3 ? rv : rk;
      const u32 nb = kvbase + (g == 3 ? 0 : (g + 1) * 131072);
      float x[16];
#pragma unroll
      for (int i = 0; i < 16; i += 2) {
        x[i] = QK(a[i]); x[i + 1] = QK(a[i + 1]);
        asm volatile("" : "+v"(x[i]), "+v"(x[i + 1]));
        BAR();
        a[i] = LD128(rn, voff, nb + i * 8192, NTL); a[i + 1] = LD128(rn, voff, nb + (i + 1) * 8192, NTL);
        SB();
      }
      SCORE()
    }
    STAMP(3);
    const float mw = wave_max(fmaxf(fmaxf(s0, s1), fmaxf(s2, s3)));
    s0 = ex2((s0 - mw) * L2E); s1 = ex2((s1 - mw) * L2E); s2 = ex2((s2 - mw) * L2E); s3 = ex2((s3 - mw) * L2E);
    const float lw = wave_sum((s0 + s1) + (s2 + s3));
    if (lane == 0) *(v2f*)(qs + 64 + 2 * w) = v2f{mw, lw};
    s0 = rbf(s0); s1 = rbf(s1); s2 = rbf(s2); s3 = rbf(s3);
    u32 e[8][8], ex[4][2];
#define WO_READ(hf) do { _Pragma("unroll") for (int k = 0; k < 4; ++k) { \
      const v4u w0 = *(const v4u*)(stg + (2 * k) * 1152 + lane * 16), w1 = *(const v4u*)(stg + (2 * k + 1) * 1152 + lane * 16); \
      u32* e_ = e[4 * (hf) + k]; \
      _Pragma("unroll") for (int d = 0; d < 4; ++d) { e_[2 * d] = plo(w0[d], w1[d]); e_[2 * d + 1] = phi(w0[d], w1[d]); } \
      asm volatile("" : "+v"(e_[0]), "+v"(e_[1]), "+v"(e_[2]), "+v"(e_[3]), "+v"(e_[4]), "+v"(e_[5]), "+v"(e_[6]), "+v"(e_[7])); } \
    _Pragma("unroll") for (int k = 0; k < 2; ++k) { \
      const char* wp = stg + ((lane >> 5) * 4 + 2 * k) * 1152 + 1024 + (lane & 31) * 4; \
      const u32 w0 = *(const u32*)wp, w1 = *(const u32*)(wp + 1152); \
      ex[2 * (hf) + k][0] = plo(w0, w1); ex[2 * (hf) + k][1] = phi(w0, w1); \
      asm volatile("" : "+v"(ex[2 * (hf) + k][0]), "+v"(ex[2 * (hf) + k][1])); } \
    asm volatile("s_waitcnt lgkmcnt(0)" ::: "memory"); SB(); } while (0)
    WO_READ(0);

    float acc[8];
#pragma unroll
    for (int d = 0; d < 8; ++d) acc[d] = 0.f;
#define PV(i, sv) do { const u32 ph_ = dppu<BC0 + (i)>(bits(sv)), pl_ = ph_ >> 16; _Pragma("unroll") for (int d = 0; d < 4; ++d) { \
      acc[2 * d] = dot2(a[i][d], pl_, acc[2 * d]); acc[2 * d + 1] = dot2(a[i][d], ph_, acc[2 * d + 1]); } \
    asm volatile("" : "+v"(acc[0]), "+v"(acc[1]), "+v"(acc[2]), "+v"(acc[3]), "+v"(acc[4]), "+v"(acc[5]), "+v"(acc[6]), "+v"(acc[7])); } while (0)
#define REP8(M) M(0) M(2) M(4) M(6) M(8) M(10) M(12) M(14)
#define PVL(i) do { if ((i) + 16 - VLAG < 16) PV(((i) + 16 - VLAG) & 15, s0); else PV(((i) + 16 - VLAG) & 15, s1); } while (0)
#define VHEAD(i) if ((i) < 16 - VLAG) { PV(i, s0); PV((i) + 1, s0); SB(); }
    REP8(VHEAD)
#undef VHEAD
#define VSTEP(i) PVL(i); PVL((i) + 1); BAR(); a[i] = LD128(rv, voff, nb + (i) * 8192, NTL); a[(i) + 1] = LD128(rv, voff, nb + ((i) + 1) * 8192, NTL); SB();
#pragma unroll 1
    for (int g = 0; g < 3; ++g) {
      const u32 nb = kvbase + (g + 1) * 131072;
      REP8(VSTEP)
      s0 = s1; s1 = s2; s2 = s3;
    }
#undef VSTEP
    STAMP(4);
    WO_READ(1);
    const float scale = ex2((mw - max8(qs[64 + 2 * (lane & 7)])) * L2E);
#define VLAST(i) if ((i) >= 16 - VLAG) { PV(i, s0); SB(); PV((i) + 1, s0); SB(); }
    REP8(VLAST)
#undef VLAST
    STAMP(5);

    {
      float* const red = img + STG_RED / 4;
      float t[4];
#pragma unroll
      for (int d = 0; d < 4; ++d) t[d] = tr32(acc[d], acc[d + 4]);
      *(v2f*)(part + w * 128 + 8 * c + 2 * r) = v2f{tr16(t[0], t[2]) * scale, tr16(t[1], t[3]) * scale};
      WG_BARRIER();
      v2f o = *(const v2f*)(part + (lane >> 3) * 128 + (lane & 4) * 16 + 8 * w + 2 * (lane & 3));
      const float F = qs[80];
      o.x += dpp<XOR8>(o.x); o.y += dpp<XOR8>(o.y);
      o.x = tr16(o.x, o.x); o.y = tr16(o.y, o.y); o.x = tr32(o.x, o.x); o.y = tr32(o.y, o.y);
      const u32 op = pk(o.x * F, o.y * F);
      float pm[8], px0 = 0.f, px1 = 0.f;
#pragma unroll
      for (int n = 0; n < 8; ++n) pm[n] = 0.f;
#define OSTEP(k) { const u32 ok_ = dppu<BC0 + (k)>(op); _Pragma("unroll") for (int n = 0; n < 8; ++n) pm[n] = dot2(e[k][n], ok_, pm[n]); }
      OSTEP(0) OSTEP(1) OSTEP(2) OSTEP(3) OSTEP(4) OSTEP(5) OSTEP(6) OSTEP(7)
#undef OSTEP
#define XSTEP(k) { u32 ok_ = __builtin_amdgcn_update_dpp(0u, op, BC0 + ((k) >> 1) * 4 + ((k) & 1), 0x3, 0xf, false); \
        ok_ = __builtin_amdgcn_update_dpp(ok_, op, BC0 + ((k) >> 1) * 4 + 2 + ((k) & 1), 0xc, 0xf, false); \
        px0 = dot2(ex[k][0], ok_, px0); px1 = dot2(ex[k][1], ok_, px1); }
      XSTEP(0) XSTEP(1) XSTEP(2) XSTEP(3)
#undef XSTEP
      const float pxs = tr32(px0, px1);
      float* rw_ = red + w * 640;
      *(v4f*)(rw_ + lane * 4) = v4f{pm[0], pm[1], pm[2], pm[3]};
      *(v4f*)(rw_ + 288 + lane * 4) = v4f{pm[4], pm[5], pm[6], pm[7]};
      rw_[576 + 2 * (lane & 31) + (lane >> 5)] = pxs;
      WG_BARRIER();
      if (w < 5) {
        const int off = tid < 256 ? ((tid >> 1) & 1) * 288 + (tid >> 2) * 4 + 2 * (tid & 1) : 576 + 2 * (tid & 31);
        v2f s = {0.f, 0.f};
#pragma unroll
        for (int i = 0; i < 8; ++i) s += *(const v2f*)(red + i * 640 + off);
        ST128((v4u{bits(s.x), tag, bits(s.y), tag}), rw, tid < 288 ? (tid >> 2) * (256 * 64) + (tid & 3) * 16 : OOB, WS_P + b * 64, SC1);
      }
    }
    STAMP(6);

    if (is_mlp) {
      BAR();
      BAR();
      u32 ge[4][8]; v4u g8;
      {
        const char* gl = (const char*)img + IMG_GW + w * 9216 + (lane & 3) * 16;
#pragma unroll
        for (int k = 0; k < 4; ++k) {
          const v4u w0 = *(const v4u*)(gl + (8 * g16 + 2 * k) * 64), w1 = *(const v4u*)(gl + (8 * g16 + 2 * k + 1) * 64);
#pragma unroll
          for (int d = 0; d < 4; ++d) { ge[k][2 * d] = plo(w0[d], w1[d]); ge[k][2 * d + 1] = phi(w0[d], w1[d]); }
        }
        g8 = *(const v4u*)(gl + (128 + g16) * 64);
      }
      const u32 hso = WS_HB + (u32)(cidx % NREP) * HB_STRIDE + 576 * (w & 3);
      u32 dw[32];
#pragma unroll
      for (int k = 0; k < 32; ++k) dw[k] = 0u;
      float Sg = 0.f, Su = 0.f, Ag = 0.f, Au = 0.f, sh = 0.f, sq = 0.f;
#pragma unroll 1
      for (int it = 0; it < 2; ++it) {
        u32 hp0 = 0x3f803f80u, hp1 = hp0, hp2 = hp0, hp3 = hp0, h8 = 0x3f80u;
        if (it) {
          const char* dl = (const char*)img + (tid < 288 ? tid : 0) * 4;
#pragma unroll
          for (int k = 0; k < 16; ++k) { const char* dp = dl + k * 2304; const u32 w0 = *(const u32*)dp, w1 = *(const u32*)(dp + 1152);
            dw[2 * k] = plo(w0, w1); dw[2 * k + 1] = phi(w0, w1); asm volatile("" : "+v"(dw[2 * k]), "+v"(dw[2 * k + 1])); }
          STAMP(1);
          v4u ga, gb, gc;
          const u32 tw = now();
          for (;;) {
            CFENCE();
            ga = LD128(rw, g16 * 32, hso, SC1); gb = LD128(rw, g16 * 32 + 16, hso, SC1); gc = LD128(rw, 512 + (g16 >> 2) * 16, hso, SC1);
            if (__all(TAGOK(ga) && TAGOK(gb) && TAGOK(gc)) || now() - tw > SPIN_TICKS) break;
          }
          hp0 = ga.x; hp1 = ga.z; hp2 = gb.x; hp3 = gb.z;
          const u32 pr = (g16 & 2) ? gc.z : gc.x;
          h8 = (g16 & 1) ? pr >> 16 : pr & 0xffffu;
          STAMP(8);
        }
        float y[8];
#pragma unroll
        for (int n = 0; n < 8; ++n) {
          float s = dot2(ge[0][n], hp0, 0.f); s = dot2(ge[1][n], hp1, s); s = dot2(ge[2][n], hp2, s); s = dot2(ge[3][n], hp3, s);
          y[n] = dot2(g8[n >> 1], (n & 1) ? h8 << 16 : h8, s);
        }
        const u32 ones = 0x3f803f80u;
        float a1 = dot2(hp0, ones, dot2(hp1, ones, dot2(hp2, ones, dot2(hp3, ones, dot2(h8, ones, 0.f)))));
        float a2 = dot2(hp0, hp0, dot2(hp1, hp1, dot2(hp2, hp2, dot2(hp3, hp3, dot2(h8, h8, 0.f)))));
        float t[4];
#pragma unroll
        for (int d = 0; d < 4; ++d) t[d] = tr32(y[d], y[d + 4]);
        const float u0 = tr16(t[0], t[2]), u1 = tr16(t[1], t[3]);
        const float v0 = u0 + dpp<XOR8>(u0), v1 = u1 + dpp<XOR8>(u1);
        float z = (lane & 8) ? v1 : v0;
        z += dpp<SHL4>(z);
        float st = tr32(a1, a2); st = tr16(st, st); st += dpp<XOR8>(st); st += dpp<SHL4>(st);
        float* gp = misc + it * 256;
        if (!(lane & 4)) gp[w * 32 + (lane & 3) * 8 + (lane >> 3)] = z;
        if ((lane & 31) == 0) misc[512 + w * 2 + (lane >> 5)] = st;
        WG_BARRIER();
        const int n = lane & 31;
        Ag = (gp[n] + gp[32 + n]) + (gp[64 + n] + gp[96 + n]);
        Au = (gp[128 + n] + gp[160 + n]) + (gp[192 + n] + gp[224 + n]);
        if (it == 0) { Sg = Ag; Su = Au; }
        else { const v4f p0 = *(const v4f*)(misc + 512), p1 = *(const v4f*)(misc + 516); sh = (p0.x + p0.z) + (p1.x + p1.z); sq = (p0.y + p0.w) + (p1.y + p1.w); }
      }
      const float mean = sh * (1.0f / D), var = fmaxf(sq * (1.0f / D) - mean * mean, 0.f);
      const float mb = rbf(mean), rs = __builtin_amdgcn_rcpf(rbf(__builtin_amdgcn_sqrtf(rbf(rbf(var) + 1e-5f))));
      const float gv = rbf((Ag - mb * Sg) * rs), uv = rbf((Au - mb * Su) * rs);
      const float zz = gv * (1.0f + 0.044715f * gv * gv) * (-2.0f * 0.7978845608028654f * L2E);
      const float gl = rbf(gv * __builtin_amdgcn_rcpf(1.0f + ex2(zz)));
      const u32 ab = bits(rbf(gl * uv));
      const u32 ap = (ab >> 16) | dppu<XOR1>(ab);
      float d0 = 0.f, d1 = 0.f;
#pragma unroll
      for (int k = 0; k < 16; ++k) { const u32 s = __builtin_amdgcn_readlane(ap, 2 * k); d0 = dot2(dw[2 * k], s, d0); d1 = dot2(dw[2 * k + 1], s, d1); }
      ST128((v4u{bits(d0), tag, bits(d1), tag}), rw, tid < 288 ? (tid >> 2) * (144 * 64) + (tid & 3) * 16 : OOB, WS_DN + cidx * 64, SC1);
      STAMP(9);
    } else if (j < 27) {
      const int rr = xcd * 9 + (j - 18);
      const v2u xv = __builtin_amdgcn_raw_buffer_load_b64(rx, (lane & 1) * 8, rr * 16, 0);
      float res0 = blo(xv.x), res1 = bhi(xv.x), res2 = blo(xv.y), res3 = bhi(xv.y);
#pragma unroll 1
      for (int it = 0; it < 2; ++it) {
        const u32 so = it ? WS_DN + rr * (144 * 64) : WS_P + rr * (256 * 64);
        const bool has2 = !it || tid < 64;
        v4u ga = {0u, 0u, 0u, 0u}, gb = ga;
        bool oka = false, okb = !has2;
        const u32 tw = now();
        for (;;) {
          CFENCE();
          if (!oka) ga = LD128(rw, tid * 16, so, SC1);
          if (!okb) gb = LD128(rw, tid * 16 + 8192, so, SC1);
          oka = TAGOK(ga); okb = !has2 || TAGOK(gb);
          if (__all(oka && okb) || now() - tw > SPIN_TICKS) break;
        }
        if (it == 0) STAMP(10); else STAMP(12);
        const float c0 = f32(ga.x) + f32(gb.x), c1 = f32(ga.z) + f32(gb.z);
        float z = tr32(c0, c1); z = tr16(z, z); z += dpp<XOR8>(z); z += dpp<SHL4>(z);
        float* gp = misc + it * 64;
        if ((lane & 31) < 4) gp[w * 8 + 2 * (lane & 3) + (lane >> 5)] = z;
        WG_BARRIER();
        if (w == 0) {
          v4f s = {0.f, 0.f, 0.f, 0.f};
#pragma unroll
          for (int i = 0; i < 8; ++i) s += *(const v4f*)(gp + i * 8 + (lane & 1) * 4);
          res0 = rbf(res0 + rbf(s.x)); res1 = rbf(res1 + rbf(s.y)); res2 = rbf(res2 + rbf(s.z)); res3 = rbf(res3 + rbf(s.w));
          const u32 p0 = (bits(res0) >> 16) | bits(res1), p1 = (bits(res2) >> 16) | bits(res3);
          if (it == 0) {
            ST128((v4u{p0, tag, p1, tag}), rw, lane < 2 * NREP ? (lane >> 1) * HB_STRIDE + (lane & 1) * 16 : OOB, WS_HB + rr * 32, SC1);
          } else {
            const rsrc_t rout = mk(pout, D * 2);
            __builtin_amdgcn_raw_buffer_store_b64(v2u{p0, p1}, rout, lane < 2 ? lane * 8 : OOB, rr * 16, 0);
            if (rr == 0) __builtin_amdgcn_raw_buffer_store_b32(tag, rw, lane == 0 ? WS_EPOCH : OOB, 0, SC1);
          }
        }
        if (it == 0) STAMP(11);
      }
    }
  }
  if (w != 8 || PROBE == 7 || PROBE == 13) { ts[14] = now(); const u32 v_ = ts[PROBE] ? ts[PROBE] - t0 : 0u; const u32 lim_ = 10000u + PROBE_MUL * v_;
    if (w != 8 && (PROBE_SEL)) while (now() - t0 < lim_) __builtin_amdgcn_s_sleep(4); }
}
}

extern "C" void kernel_launch(void* const* d_in, const int* in_sizes, int n_in, void* d_out, int out_size, void* d_ws, size_t ws_size,
                              hipStream_t stream) {
  (void)in_sizes; (void)n_in; (void)out_size; (void)ws_size;
  hipLaunchKernelGGL(fused_decode, dim3(NWG), dim3(NT), 0, stream, d_in[0], d_in[1], d_in[2], d_in[3], d_in[4], d_in[6], d_in[7], d_in[8], d_out, d_ws);
}
```

```cpp
#define PACE_T 3700
#define T1 300
#include <hip/hip_runtime.h>
#include <stdint.h>

namespace {
typedef unsigned u32;
typedef unsigned long long u64;
typedef u32 v4u __attribute__((ext_vector_type(4)));
typedef u32 v2u __attribute__((ext_vector_type(2)));
typedef float v2f __attribute__((ext_vector_type(2)));
typedef float v4f __attribute__((ext_vector_type(4)));
typedef __bf16 bf2 __attribute__((ext_vector_type(2)));
typedef __bf16 bf8 __attribute__((ext_vector_type(8)));
typedef short v4s __attribute__((ext_vector_type(4)));
typedef __amdgpu_buffer_rsrc_t rsrc_t;
#define LAS __attribute__((address_space(3)))

#define CATCH 80
#define QPOLL 400
#define CODE_BYTES 14080
#define HELP 1
#define HPOL "nt "
#define WS_BACK (32u << 20)
#define DSLEEP 16
#define ROTH 0
#define DN1 9
#define WO2_MASK 0x1F9F7F0000000000ull
#define SLOT_SH 4
constexpr int NREP = 32;

constexpr int D = 576, SK = 16384, DF = 4608;
constexpr int NWG = 256, NT = 576;
constexpr u32 KVB = 32u * SK * 128 * 2, WQB = D * 4096 * 2, WGB = D * DF * 2;
constexpr int SC1 = 16, NTL = 2;
constexpr u32 WS_EPOCH = 0;
constexpr u32 WS_ML = 4096;
constexpr u32 WS_Q = 16384;
constexpr u32 WS_P = 65536;
constexpr u32 WS_DN = WS_P + 72 * 256 * 64;
constexpr u32 HB_STRIDE = 2560;
constexpr u32 WS_HB = WS_DN + 72 * 144 * 64;
constexpr u32 WS_END = WS_HB + NREP * HB_STRIDE;
constexpr u32 OOB = 0x40000000u;
constexpr u32 SPIN_TICKS = 2000000u;
constexpr float L2E = 1.4426950408889634f;

__device__ __forceinline__ rsrc_t mk(const void* p, u32 bytes) { return __builtin_amdgcn_make_buffer_rsrc((void*)p, 0, bytes, 0x00020000); }
__device__ __forceinline__ float f32(u32 u) { return __builtin_bit_cast(float, u); }
__device__ __forceinline__ u32 bits(float f) { return __builtin_bit_cast(u32, f); }
__device__ __forceinline__ float blo(u32 u) { return f32(u << 16); }
__device__ __forceinline__ float bhi(u32 u) { return f32(u & 0xffff0000u); }
__device__ __forceinline__ float dot2(u32 a, u32 b, float c) { return __builtin_amdgcn_fdot2_f32_bf16(__builtin_bit_cast(bf2, a), __builtin_bit_cast(bf2, b), c, false); }
__device__ __forceinline__ u32 pk(float lo, float hi) { bf2 r = {(__bf16)lo, (__bf16)hi}; return __builtin_bit_cast(u32, r); }
__device__ __forceinline__ float rbf(float x) { return (float)(__bf16)x; }
__device__ __forceinline__ float ex2(float x) { return __builtin_amdgcn_exp2f(x); }
template <int CTRL> __device__ __forceinline__ float dpp(float x) { return f32(__builtin_amdgcn_mov_dpp(bits(x), CTRL, 0xf, 0xf, true)); }
template <int CTRL> __device__ __forceinline__ u32 dppu(u32 x) { return __builtin_amdgcn_mov_dpp(x, CTRL, 0xf, 0xf, true); }
constexpr int XOR1 = 0xB1, XOR2 = 0x4E, XOR7 = 0x141, XOR8 = 0x128, SHL4 = 0x104, BC0 = 0x150;
__device__ __forceinline__ float tr32(float a, float b) { auto s = __builtin_amdgcn_permlane32_swap(bits(a), bits(b), false, false); return f32(s[0]) + f32(s[1]); }
__device__ __forceinline__ float tr16(float a, float b) { auto s = __builtin_amdgcn_permlane16_swap(bits(a), bits(b), false, false); return f32(s[0]) + f32(s[1]); }
__device__ __forceinline__ float sum8(float x) { x += dpp<XOR1>(x); x += dpp<XOR2>(x); return x + dpp<XOR7>(x); }
__device__ __forceinline__ float max8(float x) { x = fmaxf(x, dpp<XOR1>(x)); x = fmaxf(x, dpp<XOR2>(x)); return fmaxf(x, dpp<XOR7>(x)); }
__device__ __forceinline__ float wave_sum(float x) { x = sum8(x); x += dpp<XOR8>(x); x = tr16(x, x); return tr32(x, x); }
__device__ __forceinline__ float wave_max(float x) {
  x = max8(x); x = fmaxf(x, dpp<XOR8>(x));
  auto s = __builtin_amdgcn_permlane16_swap(bits(x), bits(x), false, false); x = fmaxf(f32(s[0]), f32(s[1]));
  auto t = __builtin_amdgcn_permlane32_swap(bits(x), bits(x), false, false); return fmaxf(f32(t[0]), f32(t[1]));
}
__device__ __forceinline__ u32 plo(u32 a, u32 b) { return __builtin_amdgcn_perm(b, a, 0x05040100u); }
__device__ __forceinline__ u32 phi(u32 a, u32 b) { return __builtin_amdgcn_perm(b, a, 0x07060302u); }
__device__ __forceinline__ v4f mfma(v4u a, v4u b, v4f c) { return __builtin_amdgcn_mfma_f32_16x16x32_bf16(__builtin_bit_cast(bf8, a), __builtin_bit_cast(bf8, b), c, 0, 0, 0); }
__device__ __forceinline__ v4u bfrag(const char* p, int rs4) {
  const v2u lo = __builtin_bit_cast(v2u, __builtin_amdgcn_ds_read_tr16_b64_v4i16((LAS v4s*)p));
  const v2u hi = __builtin_bit_cast(v2u, __builtin_amdgcn_ds_read_tr16_b64_v4i16((LAS v4s*)(p + rs4)));
  return v4u{lo.x, lo.y, hi.x, hi.y};
}
__device__ __forceinline__ float sum4(v4f d) { const v2f s = v2f{d.x, d.y} + v2f{d.z, d.w}; return s.x + s.y; }
__device__ __forceinline__ u32 now() { return (u32)__builtin_amdgcn_s_memrealtime(); }
#define SB() __builtin_amdgcn_sched_barrier(0)
#define CFENCE() asm volatile("" ::: "memory")
#define BAR() do { CFENCE(); __builtin_amdgcn_s_barrier(); CFENCE(); } while (0)
#define WG_BARRIER() do { asm volatile("s_waitcnt lgkmcnt(0)" ::: "memory"); __builtin_amdgcn_s_barrier(); CFENCE(); } while (0)
#define DRAIN() asm volatile("s_waitcnt vmcnt(0)" ::: "memory")
#define LD128(r, v, s, aux) __builtin_amdgcn_raw_buffer_load_b128(r, v, s, aux)
#define LD32(r, v, s, aux) __builtin_amdgcn_raw_buffer_load_b32(r, v, s, aux)
#define ST128(d, r, v, s, aux) __builtin_amdgcn_raw_buffer_store_b128(d, r, v, s, aux)
#define TAGOK(g) ((g).y == tag && (g).w == tag)
#define DMA1K_(pol, ldsaddr, vo, rs, soff) do { u32 keep_; \
    asm volatile("s_mov_b32 %0, m0\n\ts_mov_b32 m0, %1\n\ts_nop 0\n\tbuffer_load_dwordx4 %2, %3, %4 offen " pol "lds\n\ts_mov_b32 m0, %0" \
                 : "=&s"(keep_) : "s"(ldsaddr), "v"(vo), "s"(rs), "s"(soff) : "memory"); } while (0)
#define DMA1K(ldsaddr, vo, rs, soff) DMA1K_("nt ", ldsaddr, vo, rs, soff)
#define DMA4K(ldsaddr, vo, rs, soff) do { u32 keep_; \
    asm volatile("s_mov_b32 %0, m0\n\ts_mov_b32 m0, %1\n\ts_nop 0\n\tbuffer_load_dwordx4 %2, %3, %4 offen nt lds\n\t" \
                 "buffer_load_dwordx4 %2, %3, %4 offen offset:1024 nt lds\n\tbuffer_load_dwordx4 %2, %3, %4 offen offset:2048 nt lds\n\t" \
                 "buffer_load_dwordx4 %2, %3, %4 offen offset:3072 nt lds\n\ts_mov_b32 m0, %0" \
                 : "=&s"(keep_) : "s"(ldsaddr), "v"(vo), "s"(rs), "s"(soff) : "memory"); } while (0)

#define STAMP(i) do { } while (0)

constexpr int IMG_GW = 73728;

__global__ void __launch_bounds__(NT, 3) fused_decode(const void* __restrict__ px, const void* __restrict__ pk_, const void* __restrict__ pv,
    const void* __restrict__ pwq, const void* __restrict__ pwo, const void* __restrict__ pwg, const void* __restrict__ pwu,
    const void* __restrict__ pwd, void* pout, void* pws) {
  __shared__ __attribute__((aligned(16))) float lds[256 + 576 + 576 + 256 + 36864];
  float* const qs = lds;
  char* const part = (char*)(lds + 256);
  char* const hs = (char*)(lds + 832);
  float* const img = lds + 1664;
  const u32 img_lds = (u32)(uintptr_t)(__attribute__((address_space(3))) float*)img;
  u64 pc0;
  asm volatile("s_getpc_b64 %0" : "=s"(pc0));
  const u32 t0 = now();
  const int b = blockIdx.x, tid = threadIdx.x, lane = tid & 63, w = __builtin_amdgcn_readfirstlane(tid >> 6);
  const int r = lane >> 4, c = lane & 15, g16 = lane >> 2;
  const int xcd = b & 7, j = b >> 3, h = xcd * 4 + (j >> 3), sp = j & 7;
  const bool is_mlp = j < 18;
  const bool is_q = j >= 24;
  const int cidx = xcd * 18 + j;
  const rsrc_t rx = mk(px, D * 2), rw = mk(pws, WS_END);
  const u32 voff = lane * 16;
  const u32 tp = t0 + T1;
#define SCHED(p) ((int)((u32)(p) * (u32)PACE_T >> 6))
#define DUE(p) do { const int due_ = SCHED(p); while ((int)(now() - tp) < due_) __builtin_amdgcn_s_sleep(1); } while (0)
  u32 tag;
#define READ_TAG() asm volatile("s_load_dword %0, %1, 0x0\n\ts_waitcnt lgkmcnt(0)\n\ts_add_u32 %0, %0, 1" : "=&s"(tag) : "s"(pws) : "scc", "memory")

  if (w == 8) {
    __builtin_amdgcn_s_setprio(3);
    const rsrc_t ro = mk(pwo, WQB);
    if (CODE_BYTES && !is_mlp) { const rsrc_t rcode = mk((const void*)(pc0 & ~255ull), CODE_BYTES); DMA1K_("", img_lds - 1024, voff, rcode, (j - 18) * 1024); }
    READ_TAG();
#pragma unroll
    for (int i = 0; i < 5; ++i) if (i < 4 || lane < 32) ((u32*)(hs + 1152))[i * 64 + lane] = 0x3f803f80u;
    if (lane == 0) *(v4f*)(qs + 84) = v4f{0.f, 0.f, 0.f, 0.f};
    if (is_q) BAR();
    {
      while (now() - t0 < QPOLL) __builtin_amdgcn_s_sleep(1);
      v4u g;
      for (;;) {
        CFENCE();
        g = LD128(rw, (lane & 31) * 16, WS_Q + h * 512, SC1);
        if (__all(TAGOK(g)) || now() - t0 > SPIN_TICKS) break;
        __builtin_amdgcn_s_sleep(8);
      }
      if (lane < 32) *(v2u*)(qs + 2 * lane) = v2u{g.x, g.z};
      STAMP(13);
    }
    WG_BARRIER();
    u32 wo_src = h * (128 * 1152), wo_dst = img_lds;
    int prev = 0;
    v4u ml = {0u, 0u, 0u, 0u};
    constexpr u64 WO_AT = 0x0000000DAD6B5B00ull | WO2_MASK;
    static_assert(__builtin_popcountll(WO_AT & 0xFFFFFFFFFFull) == 18 && __builtin_popcountll(WO_AT >> 40) == 18, "18 blocks per half");
    static_assert(__builtin_popcountll((WO_AT >> 40) & 0x1FFF) >= 9, "rows 64-95 are requested by interval 52");
#pragma unroll 1
    for (int p = 8; p < 64; ++p) {
      if (p == 39 || p == 55 || p == 63) DRAIN();
      if (p == 55) {
        if (!__all(TAGOK(ml))) {
          const u32 tw = now();
          do { __builtin_amdgcn_s_sleep(8); CFENCE(); ml = LD128(rw, (lane & 7) * 16, WS_ML + h * 256, SC1); } while (!__all(TAGOK(ml)) && now() - tw < SPIN_TICKS);
        }
        const float M = max8(f32(ml.x));
        const float L = sum8(f32(ml.z) * ex2((f32(ml.x) - M) * L2E));
        if (lane == 0) *(v2f*)(qs + 80) = v2f{M, __builtin_amdgcn_rcpf(L)};
      }
      { const int s_ = SCHED(p), m_ = prev + PACE_T * CATCH / 6400, due_ = s_ > m_ ? s_ : m_;
        while ((int)(now() - tp) < due_) __builtin_amdgcn_s_sleep(1);
        WG_BARRIER();
        const int t_ = (int)(now() - tp); prev = t_ - due_ < 8 ? due_ : t_; }
      if (p == 40) {
        const v2f v = *(const v2f*)(qs + 64 + 2 * (lane & 7));
        const float mg = max8(v.x);
        const float lg = sum8(v.y * ex2((v.x - mg) * L2E));
        ST128((v4u{bits(mg), tag, bits(lg), tag}), rw, lane == 0 ? 0u : OOB, WS_ML + h * 256 + sp * 16, SC1);
        wo_dst = img_lds;
      }
      if ((WO_AT >> p) & 1) { DMA4K(wo_dst, voff, ro, wo_src); wo_dst += 4096; wo_src += 4096; }
      if (p == 52) ml = LD128(rw, (lane & 7) * 16, WS_ML + h * 256, SC1);
    }
    if (is_mlp) {
      const rsrc_t rd = mk(pwd, WGB);
      __builtin_amdgcn_s_setprio(0);
      __builtin_amdgcn_s_sleep(2);
#pragma unroll
      for (int n = 0; n < DN1; ++n) DMA4K(img_lds + n * 4096, voff, rd, cidx * (32 * 1152) + n * 4096);
      BAR();
      if (DN1 < 9) {
        __builtin_amdgcn_s_sleep(DSLEEP);
#pragma unroll
        for (int n = DN1; n < 9; ++n) DMA4K(img_lds + n * 4096, voff, rd, cidx * (32 * 1152) + n * 4096);
      }
      DRAIN();
      BAR();
      v4u ga, gb, gc;
      const u32 tw = now();
      for (;;) {
        CFENCE();
        const u32 tn = now(), hso = WS_HB + ((tn >> SLOT_SH) & (NREP - 1)) * HB_STRIDE;
        ga = LD128(rw, voff, hso, SC1); gb = LD128(rw, voff + 1024, hso, SC1); gc = LD128(rw, lane < 16 ? voff + 2048 : OOB, hso, SC1);
        if (__all(TAGOK(ga) && TAGOK(gb) && (lane >= 16 || TAGOK(gc))) || tn - tw > SPIN_TICKS) break;
      }
      *(v2u*)(hs + lane * 8) = v2u{ga.x, ga.z}; *(v2u*)(hs + 512 + lane * 8) = v2u{gb.x, gb.z};
      if (lane < 16) *(v2u*)(hs + 1024 + lane * 8) = v2u{gc.x, gc.z};
      WG_BARRIER();
      const u32 ones = 0x3f803f80u;
      const float a1 = dot2(ga.x, ones, dot2(ga.z, ones, dot2(gb.x, ones, 0.f))) + dot2(gb.z, ones, dot2(gc.x, ones, dot2(gc.z, ones, 0.f)));
      const float a2 = dot2(ga.x, ga.x, dot2(ga.z, ga.z, dot2(gb.x, gb.x, 0.f))) + dot2(gb.z, gb.z, dot2(gc.x, gc.x, dot2(gc.z, gc.z, 0.f)));
      float st = tr32(a1, a2); st = tr16(st, st); st += dpp<XOR8>(st); st = sum8(st);
      const float mean = f32(__builtin_amdgcn_readlane(bits(st), 0)) * (1.0f / D);
      const float var = fmaxf(st * (1.0f / D) - mean * mean, 0.f);
      const float rs = __builtin_amdgcn_rcpf(rbf(__builtin_amdgcn_sqrtf(rbf(rbf(var) + 1e-5f))));
      if (lane == 32) *(v4f*)(qs + 84) = v4f{rbf(mean), rs, 1.0f, 0.f};
      asm volatile("s_waitcnt lgkmcnt(0)" ::: "memory");
    }
    __builtin_amdgcn_endpgm();
  }
  {
    const rsrc_t rk = mk(pk_, KVB), rv = mk(pv, KVB);
    const u32 kvbase = (u32)(h * 8 + sp) * 524288u + w * 1024;
#define BLK(p) (kvbase + ((((u32)(p) + (ROTH ? h : 0)) & 31u) << 14))
#define GW_DMA(k0, k1) do { const rsrc_t rg_ = mk(w < 4 ? pwg : pwu, WGB); const u32 gvo_ = g16 * 9216 + (lane & 3) * 16; \
      for (int k = (k0); k < (k1); ++k) DMA1K(img_lds + IMG_GW + w * 9216 + k * 1024, gvo_, rg_, ((w & 3) * 144 + 16 * k) * 9216 + cidx * 64); } while (0)
    if (is_mlp) GW_DMA(0, 9);
    else if (HELP && !is_q) {
      const int id = (j - 18) * 8 + w, rb = id % 9, m = id / 9;
      const void* base = m == 4 ? pwq : (m & 1) ? pwu : pwg;
      const u32 first = m == 4 ? 8 * xcd : 9 * xcd, off = (3u - ((u32)(uintptr_t)base >> 7) - first) & 7u;
      if (m < 2 || m == 4 || (m < 4 && off == 0)) {
        const u32 stride = m == 4 ? 8192 : 9216;
        const rsrc_t rh = mk(base, m == 4 ? WQB : WGB);
        const u32 vo = lane * stride, so = rb * 64 * stride + (first + off + ((m & 2) ? 8 : 0)) * 128;
        u32 keep_;
        asm volatile("s_mov_b32 %0, m0\n\ts_mov_b32 m0, %1\n\ts_nop 0\n\tbuffer_load_dword %2, %3, %4 offen " HPOL "lds\n\ts_mov_b32 m0, %0"
                     : "=&s"(keep_) : "s"(img_lds + w * 256), "v"(vo), "s"(rh), "s"(so) : "memory");
      }
    }
    if (!is_q) READ_TAG();
    else {
      const rsrc_t rq = mk(pwq, WQB);
      const int rg = lane >> 3, ch = lane & 7;
      const v4u xs0 = LD128(rx, lane * 32, 0, 0), xs1 = LD128(rx, lane * 32 + 16, 0, 0);
      u32 xr[9]; v4u wq[9];
#pragma unroll
      for (int i = 0; i < 9; ++i) xr[i] = __builtin_amdgcn_raw_buffer_load_b16(rx, rg * 18 + i * 2, 144 * w, 0);
#pragma unroll
      for (int i = 0; i < 9; ++i) wq[i] = LD128(rq, rg * (9 * 8192) + ch * 16, (72 * w + i) * 8192 + (xcd * 8 + j - 24) * 128, NTL);
      SB();
      READ_TAG();
      const u32 ones = 0x3f803f80u;
      float s = 0.f;
#pragma unroll
      for (int d = 0; d < 4; ++d) { s = dot2(xs0[d], ones, s); s = dot2(xs1[d], ones, s); }
      const float mean = wave_sum(s) * (1.0f / D);
      float vs = 0.f;
#pragma unroll
      for (int d = 0; d < 4; ++d) {
        const float e0 = blo(xs0[d]) - mean, e1 = bhi(xs0[d]) - mean, e2 = blo(xs1[d]) - mean, e3 = bhi(xs1[d]) - mean;
        vs += e0 * e0 + e1 * e1 + e2 * e2 + e3 * e3;
      }
      vs = lane < 36 ? vs : 0.f;
      const float var = wave_sum(vs) * (1.0f / D);
      const float mb = rbf(mean), sd = rbf(__builtin_amdgcn_sqrtf(rbf(rbf(var) + 1e-5f)));
      const float rs = __builtin_amdgcn_rcpf(sd);
      float y[8];
#pragma unroll
      for (int d = 0; d < 8; ++d) y[d] = 0.f;
#pragma unroll
      for (int i = 0; i < 9; ++i) {
        const u32 xh = bits(rbf(rbf(blo(xr[i]) - mb) * rs)), xl = xh >> 16;
#pragma unroll
        for (int d = 0; d < 4; ++d) { y[2 * d] = dot2(wq[i][d], xl, y[2 * d]); y[2 * d + 1] = dot2(wq[i][d], xh, y[2 * d + 1]); }
      }
      float t[4];
#pragma unroll
      for (int d = 0; d < 4; ++d) t[d] = tr32(y[d], y[d + 4]);
      const float u0 = tr16(t[0], t[2]), u1 = tr16(t[1], t[3]);
      const float v0 = u0 + dpp<XOR8>(u0), v1 = u1 + dpp<XOR8>(u1);
      ((float*)part)[w * 64 + ch * 8 + rg] = (lane & 8) ? v1 : v0;
      WG_BARRIER();
      if (w == 0) {
        float z = 0.f;
#pragma unroll
        for (int i = 0; i < 8; ++i) z += ((const float*)part)[i * 64 + lane];
        const u32 zb = bits(rbf(z)), pr = (zb >> 16) | dppu<XOR1>(zb);
        ST128((v4u{pr, tag, dppu<XOR2>(pr), tag}), rw, (lane & 3) ? OOB : lane * 4, WS_Q + (xcd * 8 + j - 24) * 256, SC1);
      }
      STAMP(7);
    }
    v4u a[16];
#pragma unroll
    for (int i = 0; i < 16; ++i) {
      if (i % 2 == 0) DUE(i / 2);
      a[i] = LD128(rk, voff, BLK(i / 2) + (i & 1) * 8192, NTL);
      SB();
    }
    BAR();
    const v4u qv = *(const v4u*)(qs + 4 * c);
    STAMP(2);

#define TR_STEP(HALF, CTRL, BIT) _Pragma("unroll") for (int k = 0; k < (HALF); ++k) { \
      const float lo_ = x[k], hi_ = x[k + (HALF)]; const float t1_ = lo_ + dpp<CTRL>(lo_), t2_ = hi_ + dpp<CTRL>(hi_); x[k] = (c & (BIT)) ? t2_ : t1_; }
#define SCORE() TR_STEP(8, XOR8, 8) TR_STEP(4, XOR7, 4) TR_STEP(2, XOR2, 2) TR_STEP(1, XOR1, 1) \
      s0 = s1; s1 = s2; s2 = s3; s3 = rbf(rbf(x[0]) * 0.08838834764831845f);
#define QK(v) dot2((v)[3], qv.w, dot2((v)[2], qv.z, dot2((v)[1], qv.y, dot2((v)[0], qv.x, 0.f))))
    float s0 = 0.f, s1 = 0.f, s2 = 0.f, s3 = 0.f;
#pragma unroll 1
    for (int g = 0; g < 4; ++g) {
      const rsrc_t rn = g == 3 ? rv : rk;
      float x[16];
#pragma unroll
      for (int i = 0; i < 16; i += 2) {
        x[i] = QK(a[i]); x[i + 1] = QK(a[i + 1]);
        asm volatile("" : "+v"(x[i]), "+v"(x[i + 1]));
        BAR();
        const u32 o = BLK(8 * g + 8 + i / 2);
        a[i] = LD128(rn, voff, o, NTL); a[i + 1] = LD128(rn, voff, o + 8192, NTL);
        SB();
      }
      SCORE()
    }
    STAMP(3);
    const float mw = wave_max(fmaxf(fmaxf(s0, s1), fmaxf(s2, s3)));
    s0 = ex2((s0 - mw) * L2E); s1 = ex2((s1 - mw) * L2E); s2 = ex2((s2 - mw) * L2E); s3 = ex2((s3 - mw) * L2E);
    const float lw = wave_sum((s0 + s1) + (s2 + s3));
    if (lane == 0) *(v2f*)(qs + 64 + 2 * w) = v2f{mw, lw};
    s0 = rbf(s0); s1 = rbf(s1); s2 = rbf(s2); s3 = rbf(s3);
    const char* const bsrc = (const char*)img + (8 * r + (c >> 2)) * 1152 + (c & 3) * 8;
    v4u bw[5][4];
#define WO_READ(ks) do { \
      _Pragma("unroll") for (int t_ = 0; t_ < 4; ++t_) bw[t_][ks] = bfrag(bsrc + w * 128 + ((ks) & 1) * (32 * 1152) + t_ * 32, 4 * 1152); \
      bw[4][ks] = bfrag(bsrc + 1024 + (w & 3) * 32 + ((ks) & 1) * (32 * 1152), 4 * 1152); \
      asm volatile("" : "+v"(bw[0][ks]), "+v"(bw[1][ks]), "+v"(bw[2][ks]), "+v"(bw[3][ks]), "+v"(bw[4][ks])); \
      asm volatile("s_waitcnt lgkmcnt(0)" ::: "memory"); SB(); } while (0)
    WO_READ(0); WO_READ(1);

    float acc[8];
#pragma unroll
    for (int d = 0; d < 8; ++d) acc[d] = 0.f;
#define PV(i, sv) do { const u32 ph_ = dppu<BC0 + (i)>(bits(sv)), pl_ = ph_ >> 16; _Pragma("unroll") for (int d = 0; d < 4; ++d) { \
      acc[2 * d] = dot2(a[i][d], pl_, acc[2 * d]); acc[2 * d + 1] = dot2(a[i][d], ph_, acc[2 * d + 1]); } \
    asm volatile("" : "+v"(acc[0]), "+v"(acc[1]), "+v"(acc[2]), "+v"(acc[3]), "+v"(acc[4]), "+v"(acc[5]), "+v"(acc[6]), "+v"(acc[7])); } while (0)
#define REP8(M) M(0) M(2) M(4) M(6) M(8) M(10) M(12) M(14)
#define VREQ(i) BAR(); { const u32 o = BLK(nb + (i) / 2); a[i] = LD128(rv, voff, o, NTL); a[(i) + 1] = LD128(rv, voff, o + 8192, NTL); } SB();
#define VSTEP(i) PV(i, s0); PV((i) + 1, s0); VREQ(i)
#pragma unroll 1
    for (int g = 0; g < 2; ++g) {
      const int nb = 8 * g + 8;
      REP8(VSTEP)
      s0 = s1; s1 = s2; s2 = s3;
    }
    WO_READ(2);
    {
      const int nb = 24;
#define VSTEP2(i) VSTEP(i) if ((i) >= 8) { PV((i) - 8, s1); PV((i) - 7, s1); SB(); }
      REP8(VSTEP2)
#undef VSTEP2
    }
#undef VSTEP
    STAMP(4);
    WO_READ(3);
    const v2f ML = *(const v2f*)(qs + 80);
    float scale = ex2((mw - ML.x) * L2E) * ML.y;
    u32 pvo = (lane & 1) ? OOB : (lane >> 3) * (256 * 64) + (lane & 6) * 8;
    LAS u32* pw = (LAS u32*)(part + w * 272 + 16 * c + 4 * r);
    const LAS char* ap_ = (const LAS char*)(part + (c & 7) * 272 + r * 16);
    asm volatile("" : "+v"(scale), "+v"(pvo), "+v"(pw), "+v"(ap_));
    const LAS char* const ap = ap_;
#define VLAST(i) if ((i) >= 8) { PV(i, s1); SB(); PV((i) + 1, s1); SB(); }
    REP8(VLAST)
#undef VLAST
    STAMP(5);

#define PUB_VO(stride) ((lane & 1) ? OOB : (lane >> 3) * (stride) + (lane & 6) * 8)
#define PUBLISH(x, y, vo, base, stride) do { \
      ST128((v4u{bits(x), tag, dppu<XOR1>(bits(x)), tag}), rw, vo, (base) + w * (8 * (stride)), SC1); \
      if (w < 4) ST128((v4u{bits(y), tag, dppu<XOR1>(bits(y)), tag}), rw, lane < 16 ? vo : OOB, (base) + (64 + 2 * w) * (stride), SC1); } while (0)
    {
      float t[4];
#pragma unroll
      for (int d = 0; d < 4; ++d) t[d] = tr32(acc[d], acc[d + 4]);
      *pw = pk(tr16(t[0], t[2]) * scale, tr16(t[1], t[3]) * scale);
      WG_BARRIER();
      v4u af[4];
#pragma unroll
      for (int k = 0; k < 4; ++k) af[k] = *(const LAS v4u*)(ap + k * 64);
      SB();
      v4f d[5];
#pragma unroll
      for (int n = 0; n < 5; ++n) d[n] = v4f{0.f, 0.f, 0.f, 0.f};
#pragma unroll
      for (int k = 0; k < 4; ++k)
#pragma unroll
        for (int n = 0; n < 4; ++n) d[n] = mfma(af[k], bw[n][k], d[n]);
      float y = 0.f;
      if (w < 4) {
#pragma unroll
        for (int k = 0; k < 4; ++k) d[4] = mfma(af[k], bw[4][k], d[4]);
        y = sum4(d[4]);
        y = tr16(y, y);
      }
      float e[4];
#pragma unroll
      for (int n = 0; n < 4; ++n) e[n] = sum4(d[n]);
      const float ab = tr16(e[0], e[1]), cd = tr16(e[2], e[3]);
      const float x = lane < 32 ? ab : cd;
      PUBLISH(x, y, pvo, WS_P + b * 64, 256 * 64);
    }
    STAMP(6);

    if (is_mlp) {
      v4u gf[18];
#define GF_READ() if (w < 4) { const char* gp = (const char*)img + IMG_GW + ((c >> 1) & 1) * 36864 + (8 * r + (c >> 2)) * 64 + w * 16 + (c & 1) * 8; \
        _Pragma("unroll") for (int k = 0; k < 18; ++k) { gf[k] = bfrag(gp + k * 2048, 256); asm volatile("" : "+v"(gf[k])); } }
      GF_READ()
      BAR();
      v4u df[5];
#pragma unroll
      for (int n = 0; n < 4; ++n) df[n] = bfrag(bsrc + w * 128 + n * 32, 4 * 1152);
      df[4] = bfrag(bsrc + 1024 + (w & 3) * 32, 4 * 1152);
      u32 dvo = (lane & 1) ? OOB : (lane >> 3) * (144 * 64) + (lane & 6) * 8;
      asm volatile("" : "+v"(df[0]), "+v"(df[1]), "+v"(df[2]), "+v"(df[3]), "+v"(df[4]), "+v"(dvo));
      STAMP(1);
      BAR();
      STAMP(8);
      if (w < 4) {
        const char* hp = hs + (c == 1 ? 1152 : 0) + r * 16;
        v4f d = {0.f, 0.f, 0.f, 0.f};
        v4u ha[18];
#pragma unroll
        for (int k = 0; k < 9; ++k) ha[k] = *(const v4u*)(hp + k * 64);
        SB();
#pragma unroll
        for (int k = 0; k < 9; ++k) { d = mfma(ha[k], gf[k], d); ha[k + 9] = *(const v4u*)(hp + (k + 9) * 64); SB(); }
#pragma unroll
        for (int k = 9; k < 18; ++k) d = mfma(ha[k], gf[k], d);
        v4f ln;
        int spins = 0;
        do { CFENCE(); ln = *(const volatile LAS v4f*)(qs + 84); } while (__builtin_amdgcn_readfirstlane(bits(ln.z)) == 0u && ++spins < (1 << 20));
        const float gv = rbf((d.x - ln.x * d.y) * ln.y), uv = dpp<XOR8>(gv);
        const float zz = gv * (1.0f + 0.044715f * gv * gv) * (-2.0f * 0.7978845608028654f * L2E);
        const float gl = rbf(gv * __builtin_amdgcn_rcpf(1.0f + ex2(zz)));
        if (lane < 8) ((unsigned short*)(qs + 96))[8 * w + lane] = (unsigned short)(bits(rbf(gl * uv)) >> 16);
      }
      WG_BARRIER();
      const v4u aa = *(const v4u*)((const char*)(qs + 96) + r * 16);
      const v4f z4 = {0.f, 0.f, 0.f, 0.f};
      const float e0 = mfma(aa, df[0], z4).x, e1 = mfma(aa, df[1], z4).x, e2 = mfma(aa, df[2], z4).x, e3 = mfma(aa, df[3], z4).x;
      float y = 0.f;
      if (w < 4) y = mfma(aa, df[4], z4).x;
      const float x = lane < 32 ? (lane < 16 ? e0 : e1) : (lane < 48 ? e2 : e3);
      PUBLISH(x, y, dvo, WS_DN + cidx * 64, 144 * 64);
      STAMP(9);
    } else if (j < 27) {
      const int rr = xcd * 9 + (j - 18);
      float res = blo((u32)__builtin_amdgcn_raw_buffer_load_b16(rx, (lane & 7) * 2, rr * 16, 0));
#pragma unroll 1
      for (int it = 0; it < 2; ++it) {
        const u32 so = it ? WS_DN + rr * (144 * 64) : WS_P + rr * (256 * 64);
        const bool has2 = !it || tid < 64;
        v4u ga = {0u, 0u, 0u, 0u}, gb = ga;
        bool oka = false, okb = !has2;
        const u32 tw = now();
        for (;;) {
          CFENCE();
          if (!oka) ga = LD128(rw, tid * 16, so, SC1);
          if (!okb) gb = LD128(rw, tid * 16 + 8192, so, SC1);
          oka = TAGOK(ga); okb = !has2 || TAGOK(gb);
          if (__all(oka && okb) || now() - tw > SPIN_TICKS) break;
        }
        if (it == 0) STAMP(10); else STAMP(12);
        const float c0 = f32(ga.x) + f32(gb.x), c1 = f32(ga.z) + f32(gb.z);
        float z = tr32(c0, c1); z = tr16(z, z); z += dpp<XOR8>(z); z += dpp<SHL4>(z);
        float* gp = qs + 128 + it * 64;
        if ((lane & 31) < 4) gp[w * 8 + 2 * (lane & 3) + (lane >> 5)] = z;
        WG_BARRIER();
        if (w == 0) {
          float s = gp[lane];
          s += dpp<XOR8>(s); s = tr16(s, s); s = tr32(s, s);
          res = rbf(res + rbf(s));
          const u32 pr = (bits(res) >> 16) | dppu<XOR1>(bits(res));
          const u32 p0 = dppu<0x00>(pr), p1 = dppu<0xAA>(pr);
          if (it == 0) {
            ST128((v4u{p0, tag, p1, tag}), rw, ((lane >> 3) * 4 + (lane & 3)) * HB_STRIDE + (lane & 4) * 4, WS_HB + rr * 32, SC1);
          } else {
            const rsrc_t rout = mk(pout, D * 2);
            __builtin_amdgcn_raw_buffer_store_b64(v2u{p0, p1}, rout, (lane & 0x3b) == 0 ? lane * 2 : OOB, rr * 16, 0);
            if (rr == 0) __builtin_amdgcn_raw_buffer_store_b32(tag, rw, lane == 0 ? WS_EPOCH : OOB, 0, SC1);
          }
        }
        if (it == 0) STAMP(11);
      }
    }
  }
}
}

extern "C" void kernel_launch(void* const* d_in, const int* in_sizes, int n_in, void* d_out, int out_size, void* d_ws, size_t ws_size,
                              hipStream_t stream) {
  (void)in_sizes; (void)n_in; (void)out_size;
  if (WS_BACK && ws_size >= (size_t)WS_BACK) d_ws = (char*)d_ws + ((ws_size - WS_BACK) & ~(size_t)4095);
  hipLaunchKernelGGL(fused_decode, dim3(NWG), dim3(NT), 0, stream, d_in[0], d_in[1], d_in[2], d_in[3], d_in[4], d_in[6], d_in[7], d_in[8], d_out, d_ws);
}
```
